```python
import torch

def _softmax(x, axis=-1):
    return torch.softmax(x, dim=axis)
def _gelu(x):
    return torch.nn.functional.gelu(x, approximate="tanh")
def _silu(x):
    return torch.nn.functional.silu(x)
def _sigmoid(x):
    return torch.sigmoid(x)
def _layernorm(x, axis=-1, eps=1e-5):
    m = x.mean(dim=axis, keepdim=True)
    v = x.var(dim=axis, keepdim=True, unbiased=False)
    return (x - m) / torch.sqrt(v + eps)
def _scatter_add(tgt, idx, src, axis=0):
    return tgt.clone().index_add_(axis, idx, src)
def _segment_sum(src, seg, n):
    out = torch.zeros((n,) + src.shape[1:], dtype=src.dtype)
    return out.index_add_(0, seg, src)
def _pad(x, pw):
    flat = tuple(v for pair in reversed(pw) for v in pair)
    return torch.nn.functional.pad(x, flat)

kernel_name = 'gated_mix__mha_block__linear_kernel_hedgehog__prompt'

def setup_inputs():

    _w = (lambda a: (lambda t: t.to(torch.bfloat16) if t.is_floating_point() else t)(torch.as_tensor(a)))
    def _A_setup():
        B, S, D, H = 1, 2048, 512, 32
        return {
            "x": _w((lambda *s: torch.randn(*s, dtype=torch.bfloat16) * 0.05)(B, S, D)),
            "wqkv": _w((lambda *s: torch.randn(*s, dtype=torch.bfloat16) * 0.8)(D, 3 * D)),
            "wo": _w((lambda *s: torch.randn(*s, dtype=torch.bfloat16) * 0.05)(D, D)),
        }
    _A_in = _A_setup()

    def _B_setup():
        B, S, D, H = 1, 2048, 512, 32
        Dh = D // H
        return {
            "x": _w((lambda *s: torch.randn(*s, dtype=torch.bfloat16) * 0.05)(B, S, D)),
            "wqkv": _w((lambda *s: torch.randn(*s, dtype=torch.bfloat16) * 1.6)(D, 3 * D)),
            "w_phi": _w((lambda *s: torch.randn(*s, dtype=torch.bfloat16) * 1.6)(H, Dh, Dh) * (Dh ** -0.5)),
            "wo": _w((lambda *s: torch.randn(*s, dtype=torch.bfloat16) * 0.05)(D, D)),
        }
    _B_in = _B_setup()

    return {
        "A_x": _A_in["x"],
        "A_wqkv": _A_in["wqkv"],
        "A_wo": _A_in["wo"],
        "B_x": _B_in["x"],
        "B_wqkv": _B_in["wqkv"],
        "B_w_phi": _B_in["w_phi"],
        "B_wo": _B_in["wo"],
        "gate_w": _w((lambda *s: torch.randn(*s, dtype=torch.bfloat16) * 0.05)(512) * 16.0),
    }

def reference(A_x, A_wqkv, A_wo, B_x, B_wqkv, B_w_phi, B_wo, gate_w):
    def _A_ref(x, wqkv, wo):
        B, S, D = x.shape
        H = 32
        Dh = D // H
        qkv = (lambda x, *d: x.permute(*d))((x @ wqkv).reshape(B, S, 3, H, Dh), 2, 0, 3, 1, 4)
        q, k, v = qkv[0], qkv[1], qkv[2]
        scores = torch.einsum("bhqd,bhkd->bhqk", q, k) * (Dh ** -0.5)
        p = _softmax(scores, axis=-1)
        o = torch.einsum("bhqk,bhkd->bhqd", p, v)
        return (lambda x, *d: x.permute(*d))(o, 0, 2, 1, 3).reshape(B, S, D) @ wo

    def _B_ref(x, wqkv, w_phi, wo):
        B, S, D = x.shape
        H = 32; Dh = D // H
        qkv = (lambda x, *d: x.permute(*d))((x @ wqkv).reshape(B, S, 3, H, Dh), 2, 0, 3, 1, 4)
        q, k, v = qkv[0], qkv[1], qkv[2]
        pq = torch.einsum("bhsd,hdf->bhsf", q, w_phi)
        pk = torch.einsum("bhsd,hdf->bhsf", k, w_phi)
        phi_q = torch.concatenate([_softmax(pq, axis=-1), _softmax(-pq, axis=-1)], axis=-1)
        phi_k = torch.concatenate([_softmax(pk, axis=-1), _softmax(-pk, axis=-1)], axis=-1)
        kv = torch.einsum("bhsf,bhsd->bhfd", phi_k, v)
        z  = torch.einsum("bhsf->bhf", phi_k)
        num = torch.einsum("bhqf,bhfd->bhqd", phi_q, kv)
        den = torch.einsum("bhqf,bhf->bhq", phi_q, z)[..., None] + 1e-6
        o = num / den
        return (lambda x, *d: x.permute(*d))(o, 0, 2, 1, 3).reshape(B, S, D) @ wo

    x = A_x
    g = _sigmoid((x * gate_w).sum(-1, keepdims=True))
    a = _A_ref(x, A_wqkv, A_wo)
    b = _B_ref(x, B_wqkv, B_w_phi, B_wo)
    return g * a + (1 - g) * b
```

```cpp
#include <hip/hip_runtime.h>
#include <hip/hip_bf16.h>
#include <stdint.h>

typedef __bf16 bf16x8 __attribute__((ext_vector_type(8)));
typedef __bf16 bf16x4 __attribute__((ext_vector_type(4)));
typedef __bf16 bf16x2 __attribute__((ext_vector_type(2)));
typedef float f32x16 __attribute__((ext_vector_type(16)));
typedef float f32x4 __attribute__((ext_vector_type(4)));
typedef float f32x2 __attribute__((ext_vector_type(2)));
typedef unsigned u32x4 __attribute__((ext_vector_type(4)));
typedef unsigned u32x2 __attribute__((ext_vector_type(2)));
#define LDS __attribute__((address_space(3)))
#define DI __device__ __forceinline__

constexpr float LOG2E = 1.4426950408889634f;

constexpr size_t WS_QA = 0;
constexpr size_t WS_KA = 2u << 20;
constexpr size_t WS_VA = 4u << 20;
constexpr size_t WS_QB = 6u << 20;
constexpr size_t WS_O  = 8u << 20;
constexpr size_t WS_KV = 12u << 20;
constexpr size_t WS_G  = 14u << 20;
constexpr int KV_PART = 2 * 17 * 16;

DI f32x16 mfma(bf16x8 a, bf16x8 b, f32x16 c) { return __builtin_amdgcn_mfma_f32_32x32x16_bf16(a, b, c, 0, 0, 0); }
DI unsigned pk2(float a, float b) {
  f32x2 v = {a, b};
  return __builtin_bit_cast(unsigned, __builtin_convertvector(v, bf16x2));
}
template <int S> DI bf16x8 pack8(const f32x16& x) {
  u32x4 p = {pk2(x[8 * S], x[8 * S + 1]), pk2(x[8 * S + 2], x[8 * S + 3]), pk2(x[8 * S + 4], x[8 * S + 5]), pk2(x[8 * S + 6], x[8 * S + 7])};
  return __builtin_bit_cast(bf16x8, p);
}
DI float bf2f(unsigned short v) { return __uint_as_float((unsigned)v << 16); }
DI float ex2(float x) { return __builtin_amdgcn_exp2f(x); }
DI float xhalf_max(float v) {
  auto r = __builtin_amdgcn_permlane32_swap(__float_as_uint(v), __float_as_uint(v), false, false);
  return fmaxf(__uint_as_float(r[0]), __uint_as_float(r[1]));
}
DI float xhalf_sum(float v) {
  auto r = __builtin_amdgcn_permlane32_swap(__float_as_uint(v), __float_as_uint(v), false, false);
  return __uint_as_float(r[0]) + __uint_as_float(r[1]);
}
DI void glds16(const void* sbase, unsigned voff, unsigned lds) {
  asm volatile("s_mov_b32 m0, %2\n\ts_nop 0\n\tglobal_load_lds_dwordx4 %0, %1" ::"v"(voff), "s"(sbase), "s"(lds) : "memory");
}
#define WAIT_VM(n) asm volatile("s_waitcnt vmcnt(" #n ")" ::: "memory")
DI void barrier() { asm volatile("s_waitcnt lgkmcnt(0)\n\ts_barrier" ::: "memory"); }
DI bf16x8 lds_frag(const LDS char* p) { return *(const LDS bf16x8*)p; }
DI bf16x8 lds_tr_frag(const LDS char* p0, const LDS char* p1) {
  bf16x4 a = __builtin_amdgcn_ds_read_tr16_b64_v4bf16((LDS bf16x4*)p0);
  bf16x4 b = __builtin_amdgcn_ds_read_tr16_b64_v4bf16((LDS bf16x4*)p1);
  return __builtin_shufflevector(a, b, 0, 1, 2, 3, 4, 5, 6, 7);
}

DI bf16x8 load_wphi(const unsigned short* wphi, int head, int lane) {
  const int r = lane & 31, h = lane >> 5;
  const unsigned sgn = (r & 16) ? 0x80008000u : 0u;
  const unsigned short* p = wphi + head * 256 + (4 * h) * 16 + (r & 15);
  u32x4 o;
#pragma unroll
  for (int j = 0; j < 4; ++j) {
    const int d0 = ((2 * j) & 3) + 8 * ((2 * j) >> 2);
    o[j] = ((unsigned)p[d0 * 16] | ((unsigned)p[(d0 + 1) * 16] << 16)) ^ sgn;
  }
  return __builtin_bit_cast(bf16x8, o);
}

DI void dual_softmax_stats(f32x16& p, float& m0, float& m1, float& s0, float& s1) {
  m0 = fmaxf(fmaxf(fmaxf(p[0], p[1]), fmaxf(p[2], p[3])), fmaxf(fmaxf(p[4], p[5]), fmaxf(p[6], p[7])));
  m1 = fmaxf(fmaxf(fmaxf(p[8], p[9]), fmaxf(p[10], p[11])), fmaxf(fmaxf(p[12], p[13]), fmaxf(p[14], p[15])));
  m0 = xhalf_max(m0);
  m1 = xhalf_max(m1);
  const float c0 = -m0 * LOG2E, c1 = -m1 * LOG2E;
#pragma unroll
  for (int i = 0; i < 8; ++i) {
    p[i] = ex2(fmaf(p[i], LOG2E, c0));
    p[8 + i] = ex2(fmaf(p[8 + i], LOG2E, c1));
  }
  s0 = ((p[0] + p[1]) + (p[2] + p[3])) + ((p[4] + p[5]) + (p[6] + p[7]));
  s1 = ((p[8] + p[9]) + (p[10] + p[11])) + ((p[12] + p[13]) + (p[14] + p[15]));
  s0 = xhalf_sum(s0);
  s1 = xhalf_sum(s1);
}

constexpr int K1_XS = 128 * 128;
constexpr int K1_WS = 64 * 384;
constexpr int K1_ST = K1_XS + K1_WS;
constexpr int K1_NS = 4;

__global__ __launch_bounds__(256, 1) void k1_qkv(const unsigned short* __restrict__ x, const unsigned short* __restrict__ wA,
                                                 const unsigned short* __restrict__ wB, const unsigned short* __restrict__ wphi,
                                                 const unsigned short* __restrict__ gw, char* __restrict__ ws) {
  __shared__ __attribute__((aligned(1024))) char smem_[K1_NS * K1_ST];
  LDS char* smem = (LDS char*)smem_;
  const unsigned lds0 = (unsigned)(uintptr_t)smem;
  const int tid = threadIdx.x, lane = tid & 63;
  const int wave = __builtin_amdgcn_readfirstlane(tid >> 6);
  const int wm = wave >> 1, wn = wave & 1;
  const int xcd = blockIdx.x & 7, idx = blockIdx.x >> 3;
  const int mb = (xcd & 1) * 8 + (idx & 7);
  const int ng = (xcd >> 1) * 4 + (idx >> 3);
  const bool isB = ng >= 8;
  const int hq = ng & 7;
  const char* Wb = (const char*)(isB ? wB : wA);
  const int r = lane & 31, h = lane >> 5;

  const int gtok = mb * 128 + ng * 8 + wave * 2;
  u32x4 gx0 = *(const u32x4*)(x + (size_t)gtok * 512 + lane * 8);
  u32x4 gx1 = *(const u32x4*)(x + (size_t)(gtok + 1) * 512 + lane * 8);
  u32x4 gwv = *(const u32x4*)(gw + lane * 8);
  bf16x8 wp0 = load_wphi(wphi, hq * 4 + wn * 2, lane);
  bf16x8 wp1 = load_wphi(wphi, hq * 4 + wn * 2 + 1, lane);

  unsigned xo[4], wo[6];
#pragma unroll
  for (int i = 0; i < 4; ++i) {
    const int row = (wave * 4 + i) * 8 + (lane >> 3);
    xo[i] = (unsigned)(mb * 128 + row) * 1024u + (unsigned)(((lane & 7) ^ ((row >> 1) & 7)) * 16);
  }
#pragma unroll
  for (int i = 0; i < 6; ++i) {
    const int bi = (wave * 6 + i) * 4 + (lane >> 4);
    const int kq = bi / 6, ct = bi % 6;
    const int k = kq * 4 + ((lane >> 2) & 3);
    const int col = (ct >> 1) * 512 + hq * 64 + (ct & 1) * 32 + (lane & 3) * 8;
    wo[i] = (unsigned)k * 3072u + (unsigned)col * 2u;
  }
  auto issue = [&](int s) {
    const unsigned base = lds0 + (s % K1_NS) * K1_ST;
    const char* xs = (const char*)x + s * 128;
    const char* wsrc = Wb + (size_t)s * 64 * 3072;
#pragma unroll
    for (int i = 0; i < 4; ++i) glds16(xs, xo[i], base + (wave * 4 + i) * 1024);
#pragma unroll
    for (int i = 0; i < 6; ++i) glds16(wsrc, wo[i], base + K1_XS + (wave * 6 + i) * 1024);
  };
#pragma unroll
  for (int s = 0; s < K1_NS; ++s) issue(s);

  f32x16 acc[2][3];
#pragma unroll
  for (int m = 0; m < 2; ++m)
#pragma unroll
    for (int c = 0; c < 3; ++c)
#pragma unroll
      for (int i = 0; i < 16; ++i) acc[m][c][i] = 0.f;

  const int xrow = wm * 64 + r;
  const int xa = xrow * 128 + ((h ^ ((xrow >> 1) & 7)) << 4);
  const int wa = K1_XS + h * 2 * 1536 + ((lane & 15) >> 2) * 64 + ((lane >> 4) & 1) * 32 + (lane & 3) * 8 + wn * 256;

#pragma unroll
  for (int s = 0; s < 8; ++s) {
    if (s == 0) WAIT_VM(30);
    else if (s <= 5) WAIT_VM(20);
    else if (s == 6) WAIT_VM(10);
    else WAIT_VM(0);
    barrier();
    if (s >= 1 && s - 1 + K1_NS < 8) issue(s - 1 + K1_NS);
    const LDS char* sb = smem + (s % K1_NS) * K1_ST;
#pragma unroll
    for (int ks = 0; ks < 4; ++ks) {
      bf16x8 xf[2], wf[3];
#pragma unroll
      for (int m = 0; m < 2; ++m) xf[m] = lds_frag(sb + ((xa ^ (ks << 5)) + m * 4096));
#pragma unroll
      for (int c = 0; c < 3; ++c) wf[c] = lds_tr_frag(sb + wa + (4 * ks) * 1536 + c * 512, sb + wa + (4 * ks + 1) * 1536 + c * 512);
#pragma unroll
      for (int m = 0; m < 2; ++m) {
        acc[m][0] = mfma(wf[0], xf[m], acc[m][0]);
        acc[m][1] = mfma(wf[1], xf[m], acc[m][1]);
        acc[m][2] = mfma(xf[m], wf[2], acc[m][2]);
      }
    }
  }

  const int head0 = hq * 4 + wn * 2;
  const int tile0 = mb * 4 + wm * 2;
  if (!isB) {
    const float qs = 0.25f * LOG2E;
#pragma unroll
    for (int m = 0; m < 2; ++m) {
      f32x16 q = acc[m][0] * qs;
      const size_t o0 = ((size_t)head0 * 64 + tile0 + m) * 1024 + lane * 16;
      *(bf16x8*)(ws + WS_QA + o0) = pack8<0>(q);
      *(bf16x8*)(ws + WS_QA + o0 + 65536) = pack8<1>(q);
      *(bf16x8*)(ws + WS_KA + o0) = pack8<0>(acc[m][1]);
      *(bf16x8*)(ws + WS_KA + o0 + 65536) = pack8<1>(acc[m][1]);
      const size_t ov = ((size_t)(head0 + (r >> 4)) * 64 + tile0 + m) * 1024 + h * 256 + (r & 15) * 16;
      *(bf16x8*)(ws + WS_VA + ov) = pack8<0>(acc[m][2]);
      *(bf16x8*)(ws + WS_VA + ov + 512) = pack8<1>(acc[m][2]);
    }
  } else {
    asm volatile("" : "+v"(wp0), "+v"(wp1));
    const u32x4 ones = {0x3F803F80u, 0x3F803F80u, 0x3F803F80u, 0x3F803F80u};
    u32x4 selv = {0u, 0u, 0u, 0u};
    if (h == 0) {
      selv[0] = r < 16 ? 0xBF80BF80u : 0u;
      selv[1] = r < 16 ? 0u : 0xBF80BF80u;
    }
    const bf16x8 sel = __builtin_bit_cast(bf16x8, selv);
    f32x16 kv[2];
#pragma unroll
    for (int i = 0; i < 16; ++i) kv[0][i] = kv[1][i] = 0.f;
    const f32x16 zero = kv[0];
#pragma unroll
    for (int m = 0; m < 2; ++m) {
      const size_t o0 = ((size_t)head0 * 64 + tile0 + m) * 1024 + lane * 16;
      *(bf16x8*)(ws + WS_QB + o0) = pack8<0>(acc[m][0]);
      *(bf16x8*)(ws + WS_QB + o0 + 65536) = pack8<1>(acc[m][0]);
      const bf16x8 v0 = pack8<0>(acc[m][2]), v1 = pack8<1>(acc[m][2]);
#pragma unroll
      for (int s = 0; s < 2; ++s) {
        const bf16x8 kf = s == 0 ? pack8<0>(acc[m][1]) : pack8<1>(acc[m][1]);
        const bf16x8 wp = s == 0 ? wp0 : wp1;
        f32x16 pt = mfma(wp, kf, zero);
        float m0, m1, s0, s1;
        dual_softmax_stats(pt, m0, m1, s0, s1);
        const float l0 = m0 + __builtin_amdgcn_logf(s0) * (1.f / LOG2E);
        const float l1 = m1 + __builtin_amdgcn_logf(s1) * (1.f / LOG2E);
        const float l0h = __uint_as_float(pk2(l0, 0.f) << 16), l1h = __uint_as_float(pk2(l1, 0.f) << 16);
        u32x4 lv = {0u, 0u, 0u, 0u};
        if (h == 0) {
          lv[0] = pk2(l0h, l0 - l0h);
          lv[1] = pk2(l1h, l1 - l1h);
        }
        f32x16 pn = mfma(kf, wp, zero);
        pn = mfma(__builtin_bit_cast(bf16x8, lv), sel, pn);
#pragma unroll
        for (int i = 0; i < 16; ++i) pn[i] = ex2(pn[i] * LOG2E);
        const bool own = (r >> 4) == s;
        const bf16x8 b0 = own ? v0 : __builtin_bit_cast(bf16x8, ones);
        const bf16x8 b1 = own ? v1 : __builtin_bit_cast(bf16x8, ones);
        kv[s] = mfma(pack8<0>(pn), b0, kv[s]);
        kv[s] = mfma(pack8<1>(pn), b1, kv[s]);
      }
    }
    barrier();
    LDS f32x4* red = (LDS f32x4*)(smem + wn * 8192);
    if (wm == 1) {
#pragma unroll
      for (int s = 0; s < 2; ++s)
#pragma unroll
        for (int g = 0; g < 4; ++g)
          red[(s * 4 + g) * 64 + lane] = f32x4{kv[s][4 * g], kv[s][4 * g + 1], kv[s][4 * g + 2], kv[s][4 * g + 3]};
    }
    barrier();
    if (wm == 0) {
#pragma unroll
      for (int s = 0; s < 2; ++s) {
        const int col = r ^ (s * 16);
        float* dst = (float*)(ws + WS_KV) + ((size_t)(head0 + s) * 16 + mb) * KV_PART + (h * 17 + col) * 16;
#pragma unroll
        for (int g = 0; g < 4; ++g) {
          const f32x4 o = red[(s * 4 + g) * 64 + lane];
          const f32x4 t = f32x4{kv[s][4 * g], kv[s][4 * g + 1], kv[s][4 * g + 2], kv[s][4 * g + 3]} + o;
          if (col <= 16) *(f32x4*)(dst + 4 * g) = t;
        }
      }
    }
  }

  asm volatile("" : "+v"(gx0), "+v"(gx1), "+v"(gwv));
  float a0 = 0.f, a1 = 0.f;
#pragma unroll
  for (int i = 0; i < 4; ++i) {
    const float wl = __uint_as_float(gwv[i] << 16), wh = __uint_as_float(gwv[i] & 0xffff0000u);
    a0 = fmaf(__uint_as_float(gx0[i] << 16), wl, a0);
    a0 = fmaf(__uint_as_float(gx0[i] & 0xffff0000u), wh, a0);
    a1 = fmaf(__uint_as_float(gx1[i] << 16), wl, a1);
    a1 = fmaf(__uint_as_float(gx1[i] & 0xffff0000u), wh, a1);
  }
#pragma unroll
  for (int o = 32; o > 0; o >>= 1) {
    a0 += __shfl_xor(a0, o);
    a1 += __shfl_xor(a1, o);
  }
  if (lane < 2) {
    const float a = lane == 0 ? a0 : a1;
    ((float*)(ws + WS_G))[gtok + lane] = 1.f / (1.f + __expf(-a));
  }
}

constexpr int K2_K = 0, K2_V = 65536, K2_ONE = 131072, K2_KV = K2_ONE + 4096, K2_LDS = K2_KV + 2048;

__global__ __launch_bounds__(512, 2) void k2_attn(const unsigned short* __restrict__ wphi, char* __restrict__ ws) {
  __shared__ __attribute__((aligned(1024))) char smem_[K2_LDS];
  LDS char* smem = (LDS char*)smem_;
  const unsigned lds0 = (unsigned)(uintptr_t)smem;
  const int tid = threadIdx.x, lane = tid & 63;
  const int wave = __builtin_amdgcn_readfirstlane(tid >> 6);
  const int xcd = blockIdx.x & 7, idx = blockIdx.x >> 3;
  const int head = xcd * 4 + (idx >> 3);
  const int tile = (idx & 7) * 8 + wave;
  const int r = lane & 31, h = lane >> 5;

  const size_t fo = ((size_t)head * 64 + tile) * 1024 + lane * 16;
  const bf16x8 qf = *(const bf16x8*)(ws + WS_QA + fo);
  const bf16x8 qb = *(const bf16x8*)(ws + WS_QB + fo);
  const float g = ((const float*)(ws + WS_G))[tile * 32 + r];
  const bf16x8 wp = load_wphi(wphi, head, lane);
  float kvs0 = 0.f, kvs1 = 0.f;
  {
    const float* kp = (const float*)(ws + WS_KV) + (size_t)head * 16 * KV_PART;
    float t0[16], t1[16];
#pragma unroll
    for (int i = 0; i < 16; ++i) {
      t0[i] = kp[i * KV_PART + tid];
      t1[i] = kp[i * KV_PART + 512 + (tid & 31)];
    }
    const char* ksrc = ws + WS_KA + (size_t)head * 65536;
    const char* vsrc = ws + WS_VA + (size_t)head * 65536;
#pragma unroll
    for (int i = 0; i < 8; ++i) {
      const unsigned off = (i * 8 + wave) * 1024;
      glds16(ksrc, off + lane * 16, lds0 + K2_K + off);
      glds16(vsrc, off + lane * 16, lds0 + K2_V + off);
    }
#pragma unroll
    for (int i = 0; i < 16; ++i) {
      kvs0 += t0[i];
      kvs1 += t1[i];
    }
  }
  *(LDS u32x2*)(smem + K2_ONE + tid * 8) = u32x2{0x3F803F80u, 0x3F803F80u};
  {
    LDS unsigned short* kvi = (LDS unsigned short*)(smem + K2_KV);
    const int e0 = tid, hh = e0 / 272, col = (e0 % 272) >> 4, reg = e0 & 15;
    const unsigned short b0 = (unsigned short)pk2(kvs0, 0.f);
    if (col < 16) kvi[(reg >> 3) * 512 + (hh * 32 + col) * 8 + (reg & 7)] = b0;
    else
      for (int c = 16; c < 32; ++c) kvi[(reg >> 3) * 512 + (hh * 32 + c) * 8 + (reg & 7)] = b0;
    if (tid < 32) {
      const int e1 = 512 + tid, col1 = (e1 % 272) >> 4, reg1 = e1 & 15;
      const unsigned short b1 = (unsigned short)pk2(kvs1, 0.f);
      if (col1 < 16) kvi[(reg1 >> 3) * 512 + (32 + col1) * 8 + (reg1 & 7)] = b1;
      else
        for (int c = 16; c < 32; ++c) kvi[(reg1 >> 3) * 512 + (32 + c) * 8 + (reg1 & 7)] = b1;
    }
  }
  WAIT_VM(0);
  barrier();

  f32x16 zero;
#pragma unroll
  for (int i = 0; i < 16; ++i) zero[i] = 0.f;
  char* obase = ws + WS_O + (size_t)tile * 65536 + lane * 16;

  {
    f32x16 p = mfma(wp, qb, zero);
    float m0, m1, s0, s1;
    dual_softmax_stats(p, m0, m1, s0, s1);
    const float r0 = 1.f / s0, r1 = 1.f / s1;
#pragma unroll
    for (int i = 0; i < 8; ++i) {
      p[i] *= r0;
      p[8 + i] *= r1;
    }
    f32x16 nd = mfma(lds_frag(smem + K2_KV + lane * 16), pack8<0>(p), zero);
    nd = mfma(lds_frag(smem + K2_KV + 1024 + lane * 16), pack8<1>(p), nd);
    const float sc = (1.f - g) / (nd[8] + 1e-6f);
    nd *= sc;
    *(bf16x8*)(obase + (32 + head) * 1024) = pack8<0>(nd);
  }

  const LDS char* kptr = smem + K2_K + lane * 16;
  const int vlane = h * 256 + (r & 15) * 16;
  f32x16 o = zero;
  {
    const LDS char* vptr = smem + (r < 16 ? K2_V : K2_ONE) + vlane;
    const int vstep = r < 16 ? 4096 : 0;
    f32x16 s = mfma(lds_frag(kptr), qf, zero);
    for (int it = 0; it < 16; ++it) {
#pragma unroll
      for (int u = 0; u < 4; ++u) {
        const int tn = (it * 4 + u + 1) & 63;
        const bf16x8 kn = lds_frag(smem + K2_K + lane * 16 + tn * 1024);
        const bf16x8 v0 = lds_frag(vptr + u * 1024), v1 = lds_frag(vptr + u * 1024 + 512);
        f32x16 sn = mfma(kn, qf, zero);
#pragma unroll
        for (int i = 0; i < 16; ++i) s[i] = ex2(s[i]);
        o = mfma(v0, pack8<0>(s), o);
        o = mfma(v1, pack8<1>(s), o);
        s = sn;
      }
      vptr += vstep;
    }
  }
  float l = o[8];
  float amax = 0.f;
#pragma unroll
  for (int i = 0; i < 8; ++i) amax = fmaxf(amax, fabsf(o[i]));
  const bool ok = l >= 0x1p-40f && l <= 0x1p60f && amax <= 0x1p120f;
  if (__builtin_expect(!__all(ok), 0)) {
    const LDS char* vptr = smem + (r < 16 ? K2_V : K2_ONE) + vlane;
    const int vstep = r < 16 ? 1024 : 0;
    float mx = -INFINITY;
    o = zero;
    for (int t = 0; t < 64; ++t) {
      f32x16 s = mfma(lds_frag(kptr + t * 1024), qf, zero);
      float tm = s[0];
#pragma unroll
      for (int i = 1; i < 16; ++i) tm = fmaxf(tm, s[i]);
      tm = xhalf_max(tm);
      const float mn = fmaxf(mx, tm);
      o *= ex2(mx - mn);
      mx = mn;
#pragma unroll
      for (int i = 0; i < 16; ++i) s[i] = ex2(s[i] - mn);
      o = mfma(lds_frag(vptr), pack8<0>(s), o);
      o = mfma(lds_frag(vptr + 512), pack8<1>(s), o);
      vptr += vstep;
    }
    l = o[8];
  }
  o *= g / l;
  *(bf16x8*)(obase + head * 1024) = pack8<0>(o);
}

constexpr int K3_LDS = 131072;

__global__ __launch_bounds__(512, 2) void k3_out(const unsigned short* __restrict__ woA, const unsigned short* __restrict__ woB,
                                                 const char* __restrict__ ws, char* __restrict__ out) {
  __shared__ __attribute__((aligned(1024))) char smem_[K3_LDS];
  LDS char* smem = (LDS char*)smem_;
  const unsigned lds0 = (unsigned)(uintptr_t)smem;
  const int tid = threadIdx.x, lane = tid & 63;
  const int wave = __builtin_amdgcn_readfirstlane(tid >> 6);
  const int kh = wave >> 2, wm = (wave >> 1) & 1, wn = wave & 1;
  const int xcd = blockIdx.x & 7, idx = blockIdx.x >> 3;
  const int mb = (xcd >> 1) * 8 + (idx & 7);
  const int nb = (xcd & 1) * 4 + (idx >> 3);
  const int r = lane & 31, h = lane >> 5;

  const char* wsrc = (const char*)(kh ? woB : woA);
  const unsigned wvo = (unsigned)(((wave & 3) * 8 + (lane >> 5) * 4 + ((lane >> 2) & 3)) * 1024 + nb * 128 + ((lane >> 4) & 1) * 64 + (lane & 3) * 16);
  const char* osrc = ws + WS_O + (size_t)(mb * 2 + wm) * 65536 + kh * 32768;
  const unsigned ovo = lane * 16;
  u32x4 of[32];
#pragma unroll
  for (int c = 0; c < 4; ++c) {
#pragma unroll
    for (int i = 0; i < 4; ++i)
      glds16(wsrc + (c * 128 + i * 32) * 1024, wvo, lds0 + kh * 65536 + c * 16384 + i * 4096 + (wave & 3) * 1024);
#pragma unroll
    for (int i = 0; i < 8; ++i)
      asm volatile("global_load_dwordx4 %0, %1, %2" : "=v"(of[c * 8 + i]) : "v"(ovo), "s"(osrc + (c * 8 + i) * 1024) : "memory");
  }
  f32x16 acc;
#pragma unroll
  for (int i = 0; i < 16; ++i) acc[i] = 0.f;
  const LDS char* wb = smem + kh * 65536 + h * 512 + wn * 256 + ((lane & 15) >> 2) * 64 + ((lane >> 4) & 1) * 32 + (lane & 3) * 8;
#pragma unroll
  for (int c = 0; c < 4; ++c) {
#define OW(n) asm volatile("s_waitcnt vmcnt(" #n ")" : "+v"(of[c * 8]), "+v"(of[c * 8 + 1]), "+v"(of[c * 8 + 2]), "+v"(of[c * 8 + 3]), \
                           "+v"(of[c * 8 + 4]), "+v"(of[c * 8 + 5]), "+v"(of[c * 8 + 6]), "+v"(of[c * 8 + 7])::"memory")
    if (c == 0) OW(36);
    else if (c == 1) OW(24);
    else if (c == 2) OW(12);
    else OW(0);
#undef OW
    barrier();
#pragma unroll
    for (int i = 0; i < 8; ++i) {
      const int ks = c * 8 + i;
      const bf16x8 wf = lds_tr_frag(wb + ks * 2048, wb + ks * 2048 + 1024);
      acc = mfma(wf, __builtin_bit_cast(bf16x8, of[ks]), acc);
    }
  }
  barrier();
  LDS f32x4* red = (LDS f32x4*)(smem + (wave & 3) * 4096);
  if (kh == 1) {
#pragma unroll
    for (int g = 0; g < 4; ++g) red[g * 64 + lane] = f32x4{acc[4 * g], acc[4 * g + 1], acc[4 * g + 2], acc[4 * g + 3]};
  }
  barrier();
  if (kh == 0) {
    unsigned pk[8];
#pragma unroll
    for (int g = 0; g < 4; ++g) {
      const f32x4 t = red[g * 64 + lane];
      pk[2 * g] = pk2(acc[4 * g] + t[0], acc[4 * g + 1] + t[1]);
      pk[2 * g + 1] = pk2(acc[4 * g + 2] + t[2], acc[4 * g + 3] + t[3]);
    }
    char* dst = out + (size_t)(mb * 64 + wm * 32 + r) * 1024 + nb * 128 + wn * 64 + h * 16;
#pragma unroll
    for (int g = 0; g < 4; g += 2) {
      auto a = __builtin_amdgcn_permlane32_swap(pk[2 * g], pk[2 * g + 2], false, false);
      auto b = __builtin_amdgcn_permlane32_swap(pk[2 * g + 1], pk[2 * g + 3], false, false);
      *(u32x4*)(dst + g * 16) = u32x4{a[0], b[0], a[1], b[1]};
    }
  }
}

extern "C" void kernel_launch(void* const* d_in, const int* in_sizes, int n_in, void* d_out, int out_size, void* d_ws,
                              size_t ws_size, hipStream_t stream) {
  const unsigned short* x = (const unsigned short*)d_in[0];
  const unsigned short* wA = (const unsigned short*)d_in[1];
  const unsigned short* woA = (const unsigned short*)d_in[2];
  const unsigned short* wB = (const unsigned short*)d_in[4];
  const unsigned short* wphi = (const unsigned short*)d_in[5];
  const unsigned short* woB = (const unsigned short*)d_in[6];
  const unsigned short* gw = (const unsigned short*)d_in[7];
  char* ws = (char*)d_ws;
  k1_qkv<<<256, 256, 0, stream>>>(x, wA, wB, wphi, gw, ws);
  k2_attn<<<256, 512, 0, stream>>>(wphi, ws);
  k3_out<<<256, 512, 0, stream>>>(woA, woB, ws, (char*)d_out);
}
```

```cpp
#include <hip/hip_runtime.h>
#include <hip/hip_bf16.h>
#include <stdint.h>

typedef __bf16 bf16x8 __attribute__((ext_vector_type(8)));
typedef __bf16 bf16x4 __attribute__((ext_vector_type(4)));
typedef __bf16 bf16x2 __attribute__((ext_vector_type(2)));
typedef float f32x16 __attribute__((ext_vector_type(16)));
typedef float f32x4 __attribute__((ext_vector_type(4)));
typedef float f32x2 __attribute__((ext_vector_type(2)));
typedef unsigned u32x4 __attribute__((ext_vector_type(4)));
typedef unsigned u32x2 __attribute__((ext_vector_type(2)));
#define LDS __attribute__((address_space(3)))
#define DI __device__ __forceinline__

constexpr float LOG2E = 1.4426950408889634f;

constexpr size_t WS_QA = 0;
constexpr size_t WS_KA = 2u << 20;
constexpr size_t WS_VA = 4u << 20;
constexpr size_t WS_QB = 6u << 20;
constexpr size_t WS_O  = 8u << 20;
constexpr size_t WS_KV = 12u << 20;
constexpr size_t WS_G  = 14u << 20;
constexpr int KV_PART = 2 * 17 * 16;

DI f32x16 mfma(bf16x8 a, bf16x8 b, f32x16 c) { return __builtin_amdgcn_mfma_f32_32x32x16_bf16(a, b, c, 0, 0, 0); }
DI unsigned pk2(float a, float b) {
  f32x2 v = {a, b};
  return __builtin_bit_cast(unsigned, __builtin_convertvector(v, bf16x2));
}
template <int S> DI bf16x8 pack8(const f32x16& x) {
  u32x4 p = {pk2(x[8 * S], x[8 * S + 1]), pk2(x[8 * S + 2], x[8 * S + 3]), pk2(x[8 * S + 4], x[8 * S + 5]), pk2(x[8 * S + 6], x[8 * S + 7])};
  return __builtin_bit_cast(bf16x8, p);
}
DI float bf2f(unsigned short v) { return __uint_as_float((unsigned)v << 16); }
DI float ex2(float x) { return __builtin_amdgcn_exp2f(x); }
DI float xhalf_max(float v) {
  auto r = __builtin_amdgcn_permlane32_swap(__float_as_uint(v), __float_as_uint(v), false, false);
  return fmaxf(__uint_as_float(r[0]), __uint_as_float(r[1]));
}
DI float xhalf_sum(float v) {
  auto r = __builtin_amdgcn_permlane32_swap(__float_as_uint(v), __float_as_uint(v), false, false);
  return __uint_as_float(r[0]) + __uint_as_float(r[1]);
}
DI void glds16(const void* sbase, unsigned voff, unsigned lds) {
  asm volatile("s_mov_b32 m0, %2\n\ts_nop 0\n\tglobal_load_lds_dwordx4 %0, %1" ::"v"(voff), "s"(sbase), "s"(lds) : "memory");
}
#define WAIT_VM(n) asm volatile("s_waitcnt vmcnt(" #n ")" ::: "memory")
DI void barrier() { asm volatile("s_waitcnt lgkmcnt(0)\n\ts_barrier" ::: "memory"); }
DI bf16x8 lds_frag(const LDS char* p) { return *(const LDS bf16x8*)p; }
DI bf16x8 lds_tr_frag(const LDS char* p0, const LDS char* p1) {
  bf16x4 a = __builtin_amdgcn_ds_read_tr16_b64_v4bf16((LDS bf16x4*)p0);
  bf16x4 b = __builtin_amdgcn_ds_read_tr16_b64_v4bf16((LDS bf16x4*)p1);
  return __builtin_shufflevector(a, b, 0, 1, 2, 3, 4, 5, 6, 7);
}

DI bf16x8 load_wphi(const unsigned short* wphi, int head, int lane) {
  const int r = lane & 31, h = lane >> 5;
  const unsigned sgn = (r & 16) ? 0x80008000u : 0u;
  const unsigned short* p = wphi + head * 256 + (4 * h) * 16 + (r & 15);
  u32x4 o;
#pragma unroll
  for (int j = 0; j < 4; ++j) {
    const int d0 = ((2 * j) & 3) + 8 * ((2 * j) >> 2);
    o[j] = ((unsigned)p[d0 * 16] | ((unsigned)p[(d0 + 1) * 16] << 16)) ^ sgn;
  }
  return __builtin_bit_cast(bf16x8, o);
}

DI void dual_softmax_stats(f32x16& p, float& m0, float& m1, float& s0, float& s1) {
  m0 = fmaxf(fmaxf(fmaxf(p[0], p[1]), fmaxf(p[2], p[3])), fmaxf(fmaxf(p[4], p[5]), fmaxf(p[6], p[7])));
  m1 = fmaxf(fmaxf(fmaxf(p[8], p[9]), fmaxf(p[10], p[11])), fmaxf(fmaxf(p[12], p[13]), fmaxf(p[14], p[15])));
  m0 = xhalf_max(m0);
  m1 = xhalf_max(m1);
  const float c0 = -m0 * LOG2E, c1 = -m1 * LOG2E;
#pragma unroll
  for (int i = 0; i < 8; ++i) {
    p[i] = ex2(fmaf(p[i], LOG2E, c0));
    p[8 + i] = ex2(fmaf(p[8 + i], LOG2E, c1));
  }
  s0 = ((p[0] + p[1]) + (p[2] + p[3])) + ((p[4] + p[5]) + (p[6] + p[7]));
  s1 = ((p[8] + p[9]) + (p[10] + p[11])) + ((p[12] + p[13]) + (p[14] + p[15]));
  s0 = xhalf_sum(s0);
  s1 = xhalf_sum(s1);
}

#define NOW(v) asm volatile("s_memrealtime %0\n\ts_waitcnt lgkmcnt(0)" : "=s"(v) :: "memory")
constexpr int K1_XS = 128 * 128;
constexpr int K1_WS = 64 * 384;
constexpr int K1_ST = K1_XS + K1_WS;
constexpr int K1_NS = 4;

__global__ __launch_bounds__(256, 1) void k1_qkv(const unsigned short* __restrict__ x, const unsigned short* __restrict__ wA,
                                                 const unsigned short* __restrict__ wB, const unsigned short* __restrict__ wphi,
                                                 const unsigned short* __restrict__ gw, char* __restrict__ ws) {
  __shared__ __attribute__((aligned(1024))) char smem_[K1_NS * K1_ST];
  LDS char* smem = (LDS char*)smem_;
  const unsigned lds0 = (unsigned)(uintptr_t)smem;
  const int tid = threadIdx.x, lane = tid & 63;
  const int wave = __builtin_amdgcn_readfirstlane(tid >> 6);
  const int wm = wave >> 1, wn = wave & 1;
  const int xcd = blockIdx.x & 7, idx = blockIdx.x >> 3;
  const int mb = (xcd & 1) * 8 + (idx & 7);
  const int ng = (xcd >> 1) * 4 + (idx >> 3);
  const bool isB = ng >= 8;
  const int hq = ng & 7;
  const char* Wb = (const char*)(isB ? wB : wA);
  const int r = lane & 31, h = lane >> 5;

  { unsigned long long t; NOW(t); if (blockIdx.x == 0 && threadIdx.x == 0) *(unsigned long long*)(ws + (20u << 20)) = t; }
  const int gtok = mb * 128 + ng * 8 + wave * 2;
  u32x4 gx0 = *(const u32x4*)(x + (size_t)gtok * 512 + lane * 8);
  u32x4 gx1 = *(const u32x4*)(x + (size_t)(gtok + 1) * 512 + lane * 8);
  u32x4 gwv = *(const u32x4*)(gw + lane * 8);
  bf16x8 wp0 = load_wphi(wphi, hq * 4 + wn * 2, lane);
  bf16x8 wp1 = load_wphi(wphi, hq * 4 + wn * 2 + 1, lane);

  unsigned xo[4], wo[6];
#pragma unroll
  for (int i = 0; i < 4; ++i) {
    const int row = (wave * 4 + i) * 8 + (lane >> 3);
    xo[i] = (unsigned)(mb * 128 + row) * 1024u + (unsigned)(((lane & 7) ^ ((row >> 1) & 7)) * 16);
  }
#pragma unroll
  for (int i = 0; i < 6; ++i) {
    const int bi = (wave * 6 + i) * 4 + (lane >> 4);
    const int kq = bi / 6, ct = bi % 6;
    const int k = kq * 4 + ((lane >> 2) & 3);
    const int col = (ct >> 1) * 512 + hq * 64 + (ct & 1) * 32 + (lane & 3) * 8;
    wo[i] = (unsigned)k * 3072u + (unsigned)col * 2u;
  }
  auto issue = [&](int s) {
    const unsigned base = lds0 + (s % K1_NS) * K1_ST;
    const char* xs = (const char*)x + s * 128;
    const char* wsrc = Wb + (size_t)s * 64 * 3072;
#pragma unroll
    for (int i = 0; i < 4; ++i) glds16(xs, xo[i], base + (wave * 4 + i) * 1024);
#pragma unroll
    for (int i = 0; i < 6; ++i) glds16(wsrc, wo[i], base + K1_XS + (wave * 6 + i) * 1024);
  };
#pragma unroll
  for (int s = 0; s < K1_NS; ++s) issue(s);

  f32x16 acc[2][3];
#pragma unroll
  for (int m = 0; m < 2; ++m)
#pragma unroll
    for (int c = 0; c < 3; ++c)
#pragma unroll
      for (int i = 0; i < 16; ++i) acc[m][c][i] = 0.f;

  const int xrow = wm * 64 + r;
  const int xa = xrow * 128 + ((h ^ ((xrow >> 1) & 7)) << 4);
  const int wa = K1_XS + h * 2 * 1536 + ((lane & 15) >> 2) * 64 + ((lane >> 4) & 1) * 32 + (lane & 3) * 8 + wn * 256;

#pragma unroll
  for (int s = 0; s < 8; ++s) {
    if (s == 0) WAIT_VM(30);
    else if (s <= 5) WAIT_VM(20);
    else if (s == 6) WAIT_VM(10);
    else WAIT_VM(0);
    barrier();
    if (s >= 1 && s - 1 + K1_NS < 8) issue(s - 1 + K1_NS);
    const LDS char* sb = smem + (s % K1_NS) * K1_ST;
#pragma unroll
    for (int ks = 0; ks < 4; ++ks) {
      bf16x8 xf[2], wf[3];
#pragma unroll
      for (int m = 0; m < 2; ++m) xf[m] = lds_frag(sb + ((xa ^ (ks << 5)) + m * 4096));
#pragma unroll
      for (int c = 0; c < 3; ++c) wf[c] = lds_tr_frag(sb + wa + (4 * ks) * 1536 + c * 512, sb + wa + (4 * ks + 1) * 1536 + c * 512);
#pragma unroll
      for (int m = 0; m < 2; ++m) {
        acc[m][0] = mfma(wf[0], xf[m], acc[m][0]);
        acc[m][1] = mfma(wf[1], xf[m], acc[m][1]);
        acc[m][2] = mfma(xf[m], wf[2], acc[m][2]);
      }
    }
  }

  const int head0 = hq * 4 + wn * 2;
  const int tile0 = mb * 4 + wm * 2;
  if (!isB) {
    const float qs = 0.25f * LOG2E;
#pragma unroll
    for (int m = 0; m < 2; ++m) {
      f32x16 q = acc[m][0] * qs;
      const size_t o0 = ((size_t)head0 * 64 + tile0 + m) * 1024 + lane * 16;
      *(bf16x8*)(ws + WS_QA + o0) = pack8<0>(q);
      *(bf16x8*)(ws + WS_QA + o0 + 65536) = pack8<1>(q);
      *(bf16x8*)(ws + WS_KA + o0) = pack8<0>(acc[m][1]);
      *(bf16x8*)(ws + WS_KA + o0 + 65536) = pack8<1>(acc[m][1]);
      const size_t ov = ((size_t)(head0 + (r >> 4)) * 64 + tile0 + m) * 1024 + h * 256 + (r & 15) * 16;
      *(bf16x8*)(ws + WS_VA + ov) = pack8<0>(acc[m][2]);
      *(bf16x8*)(ws + WS_VA + ov + 512) = pack8<1>(acc[m][2]);
    }
  } else {
    asm volatile("" : "+v"(wp0), "+v"(wp1));
    const u32x4 ones = {0x3F803F80u, 0x3F803F80u, 0x3F803F80u, 0x3F803F80u};
    u32x4 selv = {0u, 0u, 0u, 0u};
    if (h == 0) {
      selv[0] = r < 16 ? 0xBF80BF80u : 0u;
      selv[1] = r < 16 ? 0u : 0xBF80BF80u;
    }
    const bf16x8 sel = __builtin_bit_cast(bf16x8, selv);
    f32x16 kv[2];
#pragma unroll
    for (int i = 0; i < 16; ++i) kv[0][i] = kv[1][i] = 0.f;
    const f32x16 zero = kv[0];
#pragma unroll
    for (int m = 0; m < 2; ++m) {
      const size_t o0 = ((size_t)head0 * 64 + tile0 + m) * 1024 + lane * 16;
      *(bf16x8*)(ws + WS_QB + o0) = pack8<0>(acc[m][0]);
      *(bf16x8*)(ws + WS_QB + o0 + 65536) = pack8<1>(acc[m][0]);
      const bf16x8 v0 = pack8<0>(acc[m][2]), v1 = pack8<1>(acc[m][2]);
#pragma unroll
      for (int s = 0; s < 2; ++s) {
        const bf16x8 kf = s == 0 ? pack8<0>(acc[m][1]) : pack8<1>(acc[m][1]);
        const bf16x8 wp = s == 0 ? wp0 : wp1;
        f32x16 pt = mfma(wp, kf, zero);
        float m0, m1, s0, s1;
        dual_softmax_stats(pt, m0, m1, s0, s1);
        const float l0 = m0 + __builtin_amdgcn_logf(s0) * (1.f / LOG2E);
        const float l1 = m1 + __builtin_amdgcn_logf(s1) * (1.f / LOG2E);
        const float l0h = __uint_as_float(pk2(l0, 0.f) << 16), l1h = __uint_as_float(pk2(l1, 0.f) << 16);
        u32x4 lv = {0u, 0u, 0u, 0u};
        if (h == 0) {
          lv[0] = pk2(l0h, l0 - l0h);
          lv[1] = pk2(l1h, l1 - l1h);
        }
        f32x16 pn = mfma(kf, wp, zero);
        pn = mfma(__builtin_bit_cast(bf16x8, lv), sel, pn);
#pragma unroll
        for (int i = 0; i < 16; ++i) pn[i] = ex2(pn[i] * LOG2E);
        const bool own = (r >> 4) == s;
        const bf16x8 b0 = own ? v0 : __builtin_bit_cast(bf16x8, ones);
        const bf16x8 b1 = own ? v1 : __builtin_bit_cast(bf16x8, ones);
        kv[s] = mfma(pack8<0>(pn), b0, kv[s]);
        kv[s] = mfma(pack8<1>(pn), b1, kv[s]);
      }
    }
    barrier();
    LDS f32x4* red = (LDS f32x4*)(smem + wn * 8192);
    if (wm == 1) {
#pragma unroll
      for (int s = 0; s < 2; ++s)
#pragma unroll
        for (int g = 0; g < 4; ++g)
          red[(s * 4 + g) * 64 + lane] = f32x4{kv[s][4 * g], kv[s][4 * g + 1], kv[s][4 * g + 2], kv[s][4 * g + 3]};
    }
    barrier();
    if (wm == 0) {
#pragma unroll
      for (int s = 0; s < 2; ++s) {
        const int col = r ^ (s * 16);
        float* dst = (float*)(ws + WS_KV) + ((size_t)(head0 + s) * 16 + mb) * KV_PART + (h * 17 + col) * 16;
#pragma unroll
        for (int g = 0; g < 4; ++g) {
          const f32x4 o = red[(s * 4 + g) * 64 + lane];
          const f32x4 t = f32x4{kv[s][4 * g], kv[s][4 * g + 1], kv[s][4 * g + 2], kv[s][4 * g + 3]} + o;
          if (col <= 16) *(f32x4*)(dst + 4 * g) = t;
        }
      }
    }
  }

  asm volatile("" : "+v"(gx0), "+v"(gx1), "+v"(gwv));
  float a0 = 0.f, a1 = 0.f;
#pragma unroll
  for (int i = 0; i < 4; ++i) {
    const float wl = __uint_as_float(gwv[i] << 16), wh = __uint_as_float(gwv[i] & 0xffff0000u);
    a0 = fmaf(__uint_as_float(gx0[i] << 16), wl, a0);
    a0 = fmaf(__uint_as_float(gx0[i] & 0xffff0000u), wh, a0);
    a1 = fmaf(__uint_as_float(gx1[i] << 16), wl, a1);
    a1 = fmaf(__uint_as_float(gx1[i] & 0xffff0000u), wh, a1);
  }
#pragma unroll
  for (int o = 32; o > 0; o >>= 1) {
    a0 += __shfl_xor(a0, o);
    a1 += __shfl_xor(a1, o);
  }
  if (lane < 2) {
    const float a = lane == 0 ? a0 : a1;
    ((float*)(ws + WS_G))[gtok + lane] = 1.f / (1.f + __expf(-a));
  }
}

constexpr int K2_K = 0, K2_V = 65536, K2_ONE = 131072, K2_KV = K2_ONE + 4096, K2_LDS = K2_KV + 2048;

__global__ __launch_bounds__(512, 2) void k2_attn(const unsigned short* __restrict__ wphi, char* __restrict__ ws) {
  __shared__ __attribute__((aligned(1024))) char smem_[K2_LDS];
  LDS char* smem = (LDS char*)smem_;
  const unsigned lds0 = (unsigned)(uintptr_t)smem;
  const int tid = threadIdx.x, lane = tid & 63;
  const int wave = __builtin_amdgcn_readfirstlane(tid >> 6);
  const int xcd = blockIdx.x & 7, idx = blockIdx.x >> 3;
  const int head = xcd * 4 + (idx >> 3);
  const int tile = (idx & 7) * 8 + wave;
  const int r = lane & 31, h = lane >> 5;

  const size_t fo = ((size_t)head * 64 + tile) * 1024 + lane * 16;
  const bf16x8 qf = *(const bf16x8*)(ws + WS_QA + fo);
  const bf16x8 qb = *(const bf16x8*)(ws + WS_QB + fo);
  const float g = ((const float*)(ws + WS_G))[tile * 32 + r];
  const bf16x8 wp = load_wphi(wphi, head, lane);
  float kvs0 = 0.f, kvs1 = 0.f;
  {
    const float* kp = (const float*)(ws + WS_KV) + (size_t)head * 16 * KV_PART;
    float t0[16], t1[16];
#pragma unroll
    for (int i = 0; i < 16; ++i) {
      t0[i] = kp[i * KV_PART + tid];
      t1[i] = kp[i * KV_PART + 512 + (tid & 31)];
    }
    const char* ksrc = ws + WS_KA + (size_t)head * 65536;
    const char* vsrc = ws + WS_VA + (size_t)head * 65536;
#pragma unroll
    for (int i = 0; i < 8; ++i) {
      const unsigned off = (i * 8 + wave) * 1024;
      glds16(ksrc, off + lane * 16, lds0 + K2_K + off);
      glds16(vsrc, off + lane * 16, lds0 + K2_V + off);
    }
#pragma unroll
    for (int i = 0; i < 16; ++i) {
      kvs0 += t0[i];
      kvs1 += t1[i];
    }
  }
  *(LDS u32x2*)(smem + K2_ONE + tid * 8) = u32x2{0x3F803F80u, 0x3F803F80u};
  {
    LDS unsigned short* kvi = (LDS unsigned short*)(smem + K2_KV);
    const int e0 = tid, hh = e0 / 272, col = (e0 % 272) >> 4, reg = e0 & 15;
    const unsigned short b0 = (unsigned short)pk2(kvs0, 0.f);
    if (col < 16) kvi[(reg >> 3) * 512 + (hh * 32 + col) * 8 + (reg & 7)] = b0;
    else
      for (int c = 16; c < 32; ++c) kvi[(reg >> 3) * 512 + (hh * 32 + c) * 8 + (reg & 7)] = b0;
    if (tid < 32) {
      const int e1 = 512 + tid, col1 = (e1 % 272) >> 4, reg1 = e1 & 15;
      const unsigned short b1 = (unsigned short)pk2(kvs1, 0.f);
      if (col1 < 16) kvi[(reg1 >> 3) * 512 + (32 + col1) * 8 + (reg1 & 7)] = b1;
      else
        for (int c = 16; c < 32; ++c) kvi[(reg1 >> 3) * 512 + (32 + c) * 8 + (reg1 & 7)] = b1;
    }
  }
  WAIT_VM(0);
  barrier();

  f32x16 zero;
#pragma unroll
  for (int i = 0; i < 16; ++i) zero[i] = 0.f;
  char* obase = ws + WS_O + (size_t)tile * 65536 + lane * 16;

  {
    f32x16 p = mfma(wp, qb, zero);
    float m0, m1, s0, s1;
    dual_softmax_stats(p, m0, m1, s0, s1);
    const float r0 = 1.f / s0, r1 = 1.f / s1;
#pragma unroll
    for (int i = 0; i < 8; ++i) {
      p[i] *= r0;
      p[8 + i] *= r1;
    }
    f32x16 nd = mfma(lds_frag(smem + K2_KV + lane * 16), pack8<0>(p), zero);
    nd = mfma(lds_frag(smem + K2_KV + 1024 + lane * 16), pack8<1>(p), nd);
    const float sc = (1.f - g) / (nd[8] + 1e-6f);
    nd *= sc;
    *(bf16x8*)(obase + (32 + head) * 1024) = pack8<0>(nd);
  }

  const LDS char* kptr = smem + K2_K + lane * 16;
  const int vlane = h * 256 + (r & 15) * 16;
  f32x16 o = zero;
  {
    const LDS char* vptr = smem + (r < 16 ? K2_V : K2_ONE) + vlane;
    const int vstep = r < 16 ? 4096 : 0;
    f32x16 s = mfma(lds_frag(kptr), qf, zero);
    for (int it = 0; it < 16; ++it) {
#pragma unroll
      for (int u = 0; u < 4; ++u) {
        const int tn = (it * 4 + u + 1) & 63;
        const bf16x8 kn = lds_frag(smem + K2_K + lane * 16 + tn * 1024);
        const bf16x8 v0 = lds_frag(vptr + u * 1024), v1 = lds_frag(vptr + u * 1024 + 512);
        f32x16 sn = mfma(kn, qf, zero);
#pragma unroll
        for (int i = 0; i < 16; ++i) s[i] = ex2(s[i]);
        o = mfma(v0, pack8<0>(s), o);
        o = mfma(v1, pack8<1>(s), o);
        s = sn;
      }
      vptr += vstep;
    }
  }
  float l = o[8];
  float amax = 0.f;
#pragma unroll
  for (int i = 0; i < 8; ++i) amax = fmaxf(amax, fabsf(o[i]));
  const bool ok = l >= 0x1p-40f && l <= 0x1p60f && amax <= 0x1p120f;
  if (__builtin_expect(!__all(ok), 0)) {
    const LDS char* vptr = smem + (r < 16 ? K2_V : K2_ONE) + vlane;
    const int vstep = r < 16 ? 1024 : 0;
    float mx = -INFINITY;
    o = zero;
    for (int t = 0; t < 64; ++t) {
      f32x16 s = mfma(lds_frag(kptr + t * 1024), qf, zero);
      float tm = s[0];
#pragma unroll
      for (int i = 1; i < 16; ++i) tm = fmaxf(tm, s[i]);
      tm = xhalf_max(tm);
      const float mn = fmaxf(mx, tm);
      o *= ex2(mx - mn);
      mx = mn;
#pragma unroll
      for (int i = 0; i < 16; ++i) s[i] = ex2(s[i] - mn);
      o = mfma(lds_frag(vptr), pack8<0>(s), o);
      o = mfma(lds_frag(vptr + 512), pack8<1>(s), o);
      vptr += vstep;
    }
    l = o[8];
  }
  o *= g / l;
  *(bf16x8*)(obase + head * 1024) = pack8<0>(o);
}

constexpr int K3_LDS = 131072;

__global__ __launch_bounds__(512, 2) void k3_out(const unsigned short* __restrict__ woA, const unsigned short* __restrict__ woB,
                                                 const char* __restrict__ ws, char* __restrict__ out) {
  __shared__ __attribute__((aligned(1024))) char smem_[K3_LDS];
  LDS char* smem = (LDS char*)smem_;
  const unsigned lds0 = (unsigned)(uintptr_t)smem;
  const int tid = threadIdx.x, lane = tid & 63;
  const int wave = __builtin_amdgcn_readfirstlane(tid >> 6);
  const int kh = wave >> 2, wm = (wave >> 1) & 1, wn = wave & 1;
  const int xcd = blockIdx.x & 7, idx = blockIdx.x >> 3;
  const int mb = (xcd >> 1) * 8 + (idx & 7);
  const int nb = (xcd & 1) * 4 + (idx >> 3);
  const int r = lane & 31, h = lane >> 5;

  const char* wsrc = (const char*)(kh ? woB : woA);
  const unsigned wvo = (unsigned)(((wave & 3) * 8 + (lane >> 5) * 4 + ((lane >> 2) & 3)) * 1024 + nb * 128 + ((lane >> 4) & 1) * 64 + (lane & 3) * 16);
  const char* osrc = ws + WS_O + (size_t)(mb * 2 + wm) * 65536 + kh * 32768;
  const unsigned ovo = lane * 16;
  u32x4 of[32];
#pragma unroll
  for (int c = 0; c < 4; ++c) {
#pragma unroll
    for (int i = 0; i < 4; ++i)
      glds16(wsrc + (c * 128 + i * 32) * 1024, wvo, lds0 + kh * 65536 + c * 16384 + i * 4096 + (wave & 3) * 1024);
#pragma unroll
    for (int i = 0; i < 8; ++i)
      asm volatile("global_load_dwordx4 %0, %1, %2" : "=v"(of[c * 8 + i]) : "v"(ovo), "s"(osrc + (c * 8 + i) * 1024) : "memory");
  }
  f32x16 acc;
#pragma unroll
  for (int i = 0; i < 16; ++i) acc[i] = 0.f;
  const LDS char* wb = smem + kh * 65536 + h * 512 + wn * 256 + ((lane & 15) >> 2) * 64 + ((lane >> 4) & 1) * 32 + (lane & 3) * 8;
#pragma unroll
  for (int c = 0; c < 4; ++c) {
#define OW(n) asm volatile("s_waitcnt vmcnt(" #n ")" : "+v"(of[c * 8]), "+v"(of[c * 8 + 1]), "+v"(of[c * 8 + 2]), "+v"(of[c * 8 + 3]), \
                           "+v"(of[c * 8 + 4]), "+v"(of[c * 8 + 5]), "+v"(of[c * 8 + 6]), "+v"(of[c * 8 + 7])::"memory")
    if (c == 0) OW(36);
    else if (c == 1) OW(24);
    else if (c == 2) OW(12);
    else OW(0);
#undef OW
    barrier();
#pragma unroll
    for (int i = 0; i < 8; ++i) {
      const int ks = c * 8 + i;
      const bf16x8 wf = lds_tr_frag(wb + ks * 2048, wb + ks * 2048 + 1024);
      acc = mfma(wf, __builtin_bit_cast(bf16x8, of[ks]), acc);
    }
  }
  barrier();
  LDS f32x4* red = (LDS f32x4*)(smem + (wave & 3) * 4096);
  if (kh == 1) {
#pragma unroll
    for (int g = 0; g < 4; ++g) red[g * 64 + lane] = f32x4{acc[4 * g], acc[4 * g + 1], acc[4 * g + 2], acc[4 * g + 3]};
  }
  barrier();
  if (kh == 0) {
    unsigned pk[8];
#pragma unroll
    for (int g = 0; g < 4; ++g) {
      const f32x4 t = red[g * 64 + lane];
      pk[2 * g] = pk2(acc[4 * g] + t[0], acc[4 * g + 1] + t[1]);
      pk[2 * g + 1] = pk2(acc[4 * g + 2] + t[2], acc[4 * g + 3] + t[3]);
    }
    char* dst = out + (size_t)(mb * 64 + wm * 32 + r) * 1024 + nb * 128 + wn * 64 + h * 16;
#pragma unroll
    for (int g = 0; g < 4; g += 2) {
      auto a = __builtin_amdgcn_permlane32_swap(pk[2 * g], pk[2 * g + 2], false, false);
      auto b = __builtin_amdgcn_permlane32_swap(pk[2 * g + 1], pk[2 * g + 3], false, false);
      *(u32x4*)(dst + g * 16) = u32x4{a[0], b[0], a[1], b[1]};
    }
  }
  if (blockIdx.x == 0 && wave == 0) { unsigned long long e, n, t0 = *(const volatile unsigned long long*)(ws + (20u << 20)); NOW(e); e = e + 300; e = __builtin_amdgcn_readfirstlane((unsigned)e) | ((unsigned long long)__builtin_amdgcn_readfirstlane((unsigned)(e >> 32)) << 32);
    for (int k = 0; k < 20000; ++k) { NOW(n); if (n >= e) break; } }
}

extern "C" void kernel_launch(void* const* d_in, const int* in_sizes, int n_in, void* d_out, int out_size, void* d_ws,
                              size_t ws_size, hipStream_t stream) {
  const unsigned short* x = (const unsigned short*)d_in[0];
  const unsigned short* wA = (const unsigned short*)d_in[1];
  const unsigned short* woA = (const unsigned short*)d_in[2];
  const unsigned short* wB = (const unsigned short*)d_in[4];
  const unsigned short* wphi = (const unsigned short*)d_in[5];
  const unsigned short* woB = (const unsigned short*)d_in[6];
  const unsigned short* gw = (const unsigned short*)d_in[7];
  char* ws = (char*)d_ws;
  k1_qkv<<<256, 256, 0, stream>>>(x, wA, wB, wphi, gw, ws);
  k2_attn<<<256, 512, 0, stream>>>(wphi, ws);
  k3_out<<<256, 512, 0, stream>>>(woA, woB, ws, (char*)d_out);
}
```

```cpp
#include <hip/hip_runtime.h>
#include <hip/hip_bf16.h>
#include <stdint.h>

__global__ __launch_bounds__(512, 2) void fused(const void* x, const void* wqkv_a, const void* wo_a, const void* wqkv_b, const void* w_phi,
                                                const void* wo_b, const void* gate_w, void* out, void* ws) {
  __shared__ __attribute__((aligned(1024))) char smem[160 * 1024];
  unsigned tid = threadIdx.x;
  asm volatile(
    "s_load_dwordx16 s[8:23], %[ka], 0x0\n"
    "s_load_dwordx2 s[24:25], %[ka], 0x40\n"
    "s_getpc_b64 s[48:49]\n"
    "s_mov_b32 s26, %[bid]\n"
    "v_readfirstlane_b32 s27, %[tid]\n"
    "v_and_b32 v106, 63, %[tid]\n"
    "s_lshr_b32 s31, s26, 3\n"
    "s_lshl_b32 s56, s31, 4\n"
    "v_and_or_b32 v0, v106, 15, s56\n"
    "v_min_u32 v0, 251, v0\n"
    "v_lshlrev_b32 v0, 7, v0\n"
    "global_load_dword v255, v0, s[48:49]\n"
    "s_lshr_b32 s27, s27, 6\n"
    "s_and_b32 s30, s26, 7\n"
    "s_and_b32 s33, s31, 1\n"
    "s_lshr_b32 s56, s31, 3\n"
    "s_and_b32 s32, s30, 4\n"
    "s_add_u32 s32, s32, s56\n"
    "s_bfe_u32 s34, s31, 0x20001\n"
    "s_mul_i32 s35, s56, 2\n"
    "s_mul_i32 s57, s34, 2\n"
    "s_add_u32 s35, s35, s57\n"
    "s_mul_i32 s57, s33, 1\n"
    "s_add_u32 s35, s35, s57\n"
    "s_mul_i32 s57, s30, 1\n"
    "s_add_u32 s35, s35, s57\n"
    "s_and_b32 s35, s35, 7\n"
    "s_and_b32 s57, s30, 3\n"
    "s_lshl2_add_u32 s34, s57, s34\n"
    "v_lshrrev_b32 v0, 3, v106\n"
    "v_lshrrev_b32 v1, 4, v106\n"
    "v_and_b32 v2, 7, v106\n"
    "v_xor_b32 v1, v1, v2\n"
    "v_lshlrev_b32 v1, 4, v1\n"
    "v_lshl_or_b32 v96, v0, 10, v1\n"
    "v_xor_b32 v97, 0x2040, v96\n"
    "v_bfe_u32 v98, v106, 2, 2\n"
    "v_lshrrev_b32 v0, 5, v106\n"
    "v_lshl_or_b32 v98, v0, 2, v98\n"
    "v_mul_u32_u24 v98, 3072, v98\n"
    "v_bfe_u32 v0, v106, 4, 1\n"
    "v_lshl_add_u32 v98, v0, 6, v98\n"
    "v_and_b32 v0, 3, v106\n"
    "v_lshl_add_u32 v98, v0, 4, v98\n"
    "s_lshl_b32 s56, s34, 17\n"
    "s_lshl_b32 s57, s27, 14\n"
    "s_add_u32 s58, s56, s57\n"
    "s_mul_i32 s56, s27, 24576\n"
    "s_lshl_b32 s57, s32, 7\n"
    "s_add_u32 s56, s56, s57\n"
    "s_lshl_b32 s57, 0x400, s33\n"
    "s_add_u32 s59, s56, s57\n"
    "s_lshl_b32 s61, s27, 11\n"
    "s_lshl_b32 s62, s27, 10\n"
    "s_add_u32 s62, s62, 0x10000\n"
    "s_lshr_b32 s41, 0x4000, s33\n"
    "s_mov_b32 s40, s35\n"
    "s_waitcnt lgkmcnt(0)\n"
    "s_lshl_b32 s56, s40, 7\n"
    "s_add_u32 s56, s56, s58\n"
    "s_add_u32 s48, s8, s56\n"
    "s_addc_u32 s49, s9, 0\n"
    "s_mov_b32 m0, s61\n"
    "s_mul_i32 s56, s40, 196608\n"
    "global_load_lds_dwordx4 v96, s[48:49]\n"
    "s_add_u32 m0, m0, 0x400\n"
    "s_add_u32 s56, s56, s59\n"
    "global_load_lds_dwordx4 v97, s[48:49]\n"
    "s_mov_b32 m0, s62\n"
    "s_add_u32 s48, s10, s56\n"
    "s_addc_u32 s49, s11, 0\n"
    "global_load_lds_dwordx4 v98, s[48:49]\n"
    "s_cmp_lg_u32 s33, 0\n"
    "s_cbranch_scc1 E1\n"
    "s_add_u32 m0, m0, 0x2000\n"
    "s_sub_u32 s48, s48, 0x400\n"
    "s_subb_u32 s49, s49, 0\n"
    "global_load_lds_dwordx4 v98, s[48:49]\n"
    "E1:\n"
    "s_add_u32 s40, s40, 1\n"
    "s_and_b32 s40, s40, 7\n"
    "s_and_b32 s28, s27, 3\n"
    "s_lshr_b32 s29, s27, 2\n"
    "s_lshr_b32 s56, s31, 3\n"
    "s_lshl2_add_u32 s36, s30, s56\n"
    "s_and_b32 s37, s31, 7\n"
    "s_lshl2_add_u32 s38, s34, s28\n"
    "v_lshlrev_b32 v105, 4, v106\n"
    "v_mov_b32 v108, 163776\n"
    "v_mov_b32 v109, 1\n"
    "s_mov_b32 s46, 0xffff\n"
    "s_mov_b32 s47, 0xffff\n"
    "v_bfe_u32 v0, v106, 1, 3\n"
    "v_lshrrev_b32 v1, 5, v106\n"
    "v_xor_b32 v0, v0, v1\n"
    "v_lshlrev_b32 v0, 4, v0\n"
    "v_and_b32 v1, 31, v106\n"
    "v_lshl_or_b32 v0, v1, 7, v0\n"
    "s_lshl_b32 s56, s28, 12\n"
    "v_or_b32 v99, s56, v0\n"
    "v_xor_b32 v100, 32, v99\n"
    "v_xor_b32 v101, 64, v99\n"
    "v_xor_b32 v102, 0x60, v99\n"
    "v_bfe_u32 v103, v106, 2, 2\n"
    "v_lshlrev_b32 v103, 6, v103\n"
    "v_bfe_u32 v0, v106, 4, 1\n"
    "v_lshl_or_b32 v103, v0, 5, v103\n"
    "v_and_b32 v0, 3, v106\n"
    "v_lshl_or_b32 v103, v0, 3, v103\n"
    "v_lshrrev_b32 v0, 5, v106\n"
    "v_lshl_or_b32 v103, v0, 10, v103\n"
    "s_lshl_b32 s56, s40, 7\n"
    "s_add_u32 s56, s56, s58\n"
    "s_add_u32 s48, s8, s56\n"
    "s_addc_u32 s49, s9, 0\n"
    "s_add_u32 m0, s61, 0x4000\n"
    "s_mul_i32 s56, s40, 196608\n"
    "global_load_lds_dwordx4 v96, s[48:49]\n"
    "s_add_u32 m0, m0, 0x400\n"
    "s_add_u32 s56, s56, s59\n"
    "global_load_lds_dwordx4 v97, s[48:49]\n"
    "s_add_u32 m0, s62, s41\n"
    "s_add_u32 s48, s10, s56\n"
    "s_addc_u32 s49, s11, 0\n"
    "global_load_lds_dwordx4 v98, s[48:49]\n"
    "s_cmp_lg_u32 s33, 0\n"
    "s_cbranch_scc1 E2\n"
    "s_add_u32 m0, m0, 0x2000\n"
    "s_sub_u32 s48, s48, 0x400\n"
    "s_subb_u32 s49, s49, 0\n"
    "global_load_lds_dwordx4 v98, s[48:49]\n"
    "E2:\n"
    "s_add_u32 s48, s24, 18874368\n"
    "s_addc_u32 s49, s25, 0\n"
    "v_mov_b32 v0, 0\n"
    "global_load_dword v104, v0, s[48:49] sc1\n"
    "s_getpc_b64 s[42:43]\n"
    "TABREF:\n"
    "s_lshl2_add_u32 s56, s28, PADTAB-TABREF\n"
    "s_add_u32 s42, s42, s56\n"
    "s_addc_u32 s43, s43, 0\n"
    "s_lshl1_add_u32 s56, s29, s33\n"
    "s_cmp_eq_u32 s56, 0\n"
    "s_cbranch_scc1 A0_\n"
    "s_cmp_eq_u32 s56, 1\n"
    "s_cbranch_scc1 A1_\n"
    "s_cmp_eq_u32 s56, 3\n"
    "s_cbranch_scc1 B1_\n"
    "s_branch B0_\n"
    "PADTAB:\n"
    "s_branch 10f\n"
    "s_branch 11f\n"
    "s_branch 12f\n"
    "s_branch 13f\n"
    "s_branch 14f\n"
    "s_branch 15f\n"
    "s_branch 16f\n"
    "s_branch 17f\n"
    "s_branch 22f\n"
    "s_branch 23f\n"
    "s_branch 24f\n"
    "s_branch 25f\n"
    "s_branch 26f\n"
    "s_branch 27f\n"
    "s_branch 28f\n"
    "s_branch 29f\n"
    "s_branch 30f\n"
    "s_branch 31f\n"
    "s_branch 32f\n"
    "s_branch 33f\n"
    "s_branch 34f\n"
    "s_branch 35f\n"
    "s_branch 36f\n"
    "s_branch 37f\n"
    "B0_:\n"
    "s_lshl_b32 s56, s38, 15\n"
    "s_add_u32 s64, s8, s56\n"
    "s_addc_u32 s65, s9, 0\n"
    "s_mul_i32 s56, s28, 49152\n"
    "s_lshl_b32 s57, s32, 7\n"
    "s_add_u32 s56, s56, s57\n"
    "s_add_u32 s57, s56, 0\n"
    "s_add_u32 s66, s10, s57\n"
    "s_addc_u32 s67, s11, 0\n"
    "s_add_u32 s57, s56, 0\n"
    "s_add_u32 s68, s14, s57\n"
    "s_addc_u32 s69, s15, 0\n"
    "s_lshl_b32 s61, s28, 12\n"
    "s_lshl_b32 s62, s28, 11\n"
    "v_add_u32 v125, 0x10000, v103\n"
    "v_add_u32 v126, 0x20000, v103\n"
    "s_add_u32 s57, s35, 2\n"
    "s_and_b32 s57, s57, 7\n"
    "s_lshl_b32 s56, s57, 7\n"
    "s_add_u32 s48, s64, s56\n"
    "s_addc_u32 s49, s65, 0\n"
    "s_add_u32 m0, s61, 32768\n"
    "s_nop 0\n"
    "global_load_lds_dwordx4 v96, s[48:49]\n"
    "s_add_u32 m0, s61, 33792\n"
    "s_nop 0\n"
    "global_load_lds_dwordx4 v97, s[48:49]\n"
    "s_add_u32 m0, s61, 34816\n"
    "s_add_u32 s48, s48, 16384\n"
    "s_addc_u32 s49, s49, 0\n"
    "global_load_lds_dwordx4 v96, s[48:49]\n"
    "s_add_u32 m0, s61, 35840\n"
    "s_nop 0\n"
    "global_load_lds_dwordx4 v97, s[48:49]\n"
    "s_mul_i32 s58, s57, 196608\n"
    "s_add_u32 m0, s62, 98304\n"
    "s_add_u32 s56, s58, 1024\n"
    "s_add_u32 s48, s66, s56\n"
    "s_addc_u32 s49, s67, 0\n"
    "global_load_lds_dwordx4 v98, s[48:49]\n"
    "s_add_u32 m0, s62, 99328\n"
    "s_add_u32 s56, s58, 25600\n"
    "s_add_u32 s48, s66, s56\n"
    "s_addc_u32 s49, s67, 0\n"
    "global_load_lds_dwordx4 v98, s[48:49]\n"
    "s_add_u32 m0, s62, 106496\n"
    "s_add_u32 s56, s58, 0\n"
    "s_add_u32 s48, s66, s56\n"
    "s_addc_u32 s49, s67, 0\n"
    "global_load_lds_dwordx4 v98, s[48:49]\n"
    "s_add_u32 m0, s62, 107520\n"
    "s_add_u32 s56, s58, 24576\n"
    "s_add_u32 s48, s66, s56\n"
    "s_addc_u32 s49, s67, 0\n"
    "global_load_lds_dwordx4 v98, s[48:49]\n"
    "s_waitcnt vmcnt(13) lgkmcnt(0)\n"
    "s_barrier\n"
    "s_add_u32 s57, s35, 3\n"
    "s_and_b32 s57, s57, 7\n"
    "s_lshl_b32 s56, s57, 7\n"
    "s_add_u32 s48, s64, s56\n"
    "s_addc_u32 s49, s65, 0\n"
    "s_add_u32 m0, s61, 49152\n"
    "s_nop 0\n"
    "global_load_lds_dwordx4 v96, s[48:49]\n"
    "s_add_u32 m0, s61, 50176\n"
    "s_nop 0\n"
    "global_load_lds_dwordx4 v97, s[48:49]\n"
    "s_add_u32 m0, s61, 51200\n"
    "s_add_u32 s48, s48, 16384\n"
    "s_addc_u32 s49, s49, 0\n"
    "global_load_lds_dwordx4 v96, s[48:49]\n"
    "s_add_u32 m0, s61, 52224\n"
    "s_nop 0\n"
    "global_load_lds_dwordx4 v97, s[48:49]\n"
    "s_mul_i32 s58, s57, 196608\n"
    "s_add_u32 m0, s62, 114688\n"
    "s_add_u32 s56, s58, 1024\n"
    "s_add_u32 s48, s66, s56\n"
    "s_addc_u32 s49, s67, 0\n"
    "global_load_lds_dwordx4 v98, s[48:49]\n"
    "s_add_u32 m0, s62, 115712\n"
    "s_add_u32 s56, s58, 25600\n"
    "s_add_u32 s48, s66, s56\n"
    "s_addc_u32 s49, s67, 0\n"
    "global_load_lds_dwordx4 v98, s[48:49]\n"
    "s_add_u32 m0, s62, 122880\n"
    "s_add_u32 s56, s58, 0\n"
    "s_add_u32 s48, s66, s56\n"
    "s_addc_u32 s49, s67, 0\n"
    "global_load_lds_dwordx4 v98, s[48:49]\n"
    "s_add_u32 m0, s62, 123904\n"
    "s_add_u32 s56, s58, 24576\n"
    "s_add_u32 s48, s66, s56\n"
    "s_addc_u32 s49, s67, 0\n"
    "global_load_lds_dwordx4 v98, s[48:49]\n"
    "ds_read_b128 v[128:131], v99 offset:0\n"
    "ds_read_b128 v[132:135], v100 offset:0\n"
    "ds_read_b128 v[136:139], v101 offset:0\n"
    "ds_read_b128 v[140:143], v102 offset:0\n"
    "s_and_b32 s56, s28, 1\n"
    "s_lshl_b32 s56, s56, 10\n"
    "s_add_u32 m0, s56, 157696\n"
    "s_lshl_b32 s57, s32, 11\n"
    "s_add_u32 s56, s56, s57\n"
    "s_add_u32 s48, s16, s56\n"
    "s_addc_u32 s49, s17, 0\n"
    "global_load_lds_dwordx4 v105, s[48:49]\n"
    "s_lshl1_add_u32 s56, s32, s33\n"
    "s_lshl2_add_u32 s56, s56, s28\n"
    "s_lshl_b32 s57, s34, 6\n"
    "s_add_u32 s71, s56, s57\n"
    "s_lshl_b32 s56, s71, 11\n"
    "s_add_u32 s48, s8, s56\n"
    "s_addc_u32 s49, s9, 0\n"
    "global_load_dwordx4 v[112:115], v105, s[48:49]\n"
    "global_load_dwordx4 v[116:119], v105, s[48:49] offset:1024\n"
    "global_load_dwordx4 v[120:123], v105, s[20:21]\n"
    "v_bfe_u32 v124, v106, 2, 2\n"
    "v_lshrrev_b32 v0, 5, v106\n"
    "v_lshl_or_b32 v124, v0, 2, v124\n"
    "v_lshlrev_b32 v124, 5, v124\n"
    "v_and_b32 v0, 3, v106\n"
    "v_lshl_or_b32 v124, v0, 3, v124\n"
    "v_add_u32 v124, 157696, v124\n"
    "s_waitcnt vmcnt(21) lgkmcnt(0)\n"
    "s_barrier\n"
    "s_add_u32 s57, s35, 4\n"
    "s_and_b32 s57, s57, 7\n"
    "s_lshl_b32 s56, s57, 7\n"
    "s_add_u32 s48, s64, s56\n"
    "s_addc_u32 s49, s65, 0\n"
    "s_add_u32 m0, s61, 0\n"
    "s_nop 0\n"
    "global_load_lds_dwordx4 v96, s[48:49]\n"
    "s_add_u32 m0, s61, 1024\n"
    "s_nop 0\n"
    "global_load_lds_dwordx4 v97, s[48:49]\n"
    "s_add_u32 m0, s61, 2048\n"
    "s_add_u32 s48, s48, 16384\n"
    "s_addc_u32 s49, s49, 0\n"
    "global_load_lds_dwordx4 v96, s[48:49]\n"
    "s_add_u32 m0, s61, 3072\n"
    "s_nop 0\n"
    "global_load_lds_dwordx4 v97, s[48:49]\n"
    "s_mul_i32 s58, s57, 196608\n"
    "s_add_u32 m0, s62, 65536\n"
    "s_add_u32 s56, s58, 1024\n"
    "s_add_u32 s48, s66, s56\n"
    "s_addc_u32 s49, s67, 0\n"
    "global_load_lds_dwordx4 v98, s[48:49]\n"
    "s_add_u32 m0, s62, 66560\n"
    "s_add_u32 s56, s58, 25600\n"
    "s_add_u32 s48, s66, s56\n"
    "s_addc_u32 s49, s67, 0\n"
    "global_load_lds_dwordx4 v98, s[48:49]\n"
    "s_add_u32 m0, s62, 73728\n"
    "s_add_u32 s56, s58, 0\n"
    "s_add_u32 s48, s66, s56\n"
    "s_addc_u32 s49, s67, 0\n"
    "global_load_lds_dwordx4 v98, s[48:49]\n"
    "s_add_u32 m0, s62, 74752\n"
    "s_add_u32 s56, s58, 24576\n"
    "s_add_u32 s48, s66, s56\n"
    "s_addc_u32 s49, s67, 0\n"
    "global_load_lds_dwordx4 v98, s[48:49]\n"
    "ds_read_b128 v[144:147], v99 offset:16384\n"
    "ds_read_b128 v[148:151], v100 offset:16384\n"
    "ds_read_b128 v[152:155], v101 offset:16384\n"
    "ds_read_b128 v[156:159], v102 offset:16384\n"
    "s_waitcnt vmcnt(20) lgkmcnt(0)\n"
    "s_barrier\n"
    "s_add_u32 s57, s35, 5\n"
    "s_and_b32 s57, s57, 7\n"
    "s_lshl_b32 s56, s57, 7\n"
    "s_add_u32 s48, s64, s56\n"
    "s_addc_u32 s49, s65, 0\n"
    "s_add_u32 m0, s61, 16384\n"
    "s_nop 0\n"
    "global_load_lds_dwordx4 v96, s[48:49]\n"
    "s_add_u32 m0, s61, 17408\n"
    "s_nop 0\n"
    "global_load_lds_dwordx4 v97, s[48:49]\n"
    "s_add_u32 m0, s61, 18432\n"
    "s_add_u32 s48, s48, 16384\n"
    "s_addc_u32 s49, s49, 0\n"
    "global_load_lds_dwordx4 v96, s[48:49]\n"
    "s_add_u32 m0, s61, 19456\n"
    "s_nop 0\n"
    "global_load_lds_dwordx4 v97, s[48:49]\n"
    "s_mul_i32 s58, s57, 196608\n"
    "s_add_u32 m0, s62, 81920\n"
    "s_add_u32 s56, s58, 1024\n"
    "s_add_u32 s48, s66, s56\n"
    "s_addc_u32 s49, s67, 0\n"
    "global_load_lds_dwordx4 v98, s[48:49]\n"
    "s_add_u32 m0, s62, 82944\n"
    "s_add_u32 s56, s58, 25600\n"
    "s_add_u32 s48, s66, s56\n"
    "s_addc_u32 s49, s67, 0\n"
    "global_load_lds_dwordx4 v98, s[48:49]\n"
    "s_add_u32 m0, s62, 90112\n"
    "s_add_u32 s56, s58, 0\n"
    "s_add_u32 s48, s66, s56\n"
    "s_addc_u32 s49, s67, 0\n"
    "global_load_lds_dwordx4 v98, s[48:49]\n"
    "s_add_u32 m0, s62, 91136\n"
    "s_add_u32 s56, s58, 24576\n"
    "s_add_u32 s48, s66, s56\n"
    "s_addc_u32 s49, s67, 0\n"
    "global_load_lds_dwordx4 v98, s[48:49]\n"
    "ds_read_b128 v[160:163], v99 offset:32768\n"
    "ds_read_b128 v[164:167], v100 offset:32768\n"
    "ds_read_b128 v[168:171], v101 offset:32768\n"
    "ds_read_b128 v[172:175], v102 offset:32768\n"
    "s_waitcnt vmcnt(20) lgkmcnt(0)\n"
    "s_barrier\n"
    "s_add_u32 s57, s35, 6\n"
    "s_and_b32 s57, s57, 7\n"
    "s_lshl_b32 s56, s57, 7\n"
    "s_add_u32 s48, s64, s56\n"
    "s_addc_u32 s49, s65, 0\n"
    "s_add_u32 m0, s61, 32768\n"
    "s_nop 0\n"
    "global_load_lds_dwordx4 v96, s[48:49]\n"
    "s_add_u32 m0, s61, 33792\n"
    "s_nop 0\n"
    "global_load_lds_dwordx4 v97, s[48:49]\n"
    "s_add_u32 m0, s61, 34816\n"
    "s_add_u32 s48, s48, 16384\n"
    "s_addc_u32 s49, s49, 0\n"
    "global_load_lds_dwordx4 v96, s[48:49]\n"
    "s_add_u32 m0, s61, 35840\n"
    "s_nop 0\n"
    "global_load_lds_dwordx4 v97, s[48:49]\n"
    "s_mul_i32 s58, s57, 196608\n"
    "s_add_u32 m0, s62, 98304\n"
    "s_add_u32 s56, s58, 1024\n"
    "s_add_u32 s48, s66, s56\n"
    "s_addc_u32 s49, s67, 0\n"
    "global_load_lds_dwordx4 v98, s[48:49]\n"
    "s_add_u32 m0, s62, 99328\n"
    "s_add_u32 s56, s58, 25600\n"
    "s_add_u32 s48, s66, s56\n"
    "s_addc_u32 s49, s67, 0\n"
    "global_load_lds_dwordx4 v98, s[48:49]\n"
    "s_add_u32 m0, s62, 106496\n"
    "s_add_u32 s56, s58, 0\n"
    "s_add_u32 s48, s66, s56\n"
    "s_addc_u32 s49, s67, 0\n"
    "global_load_lds_dwordx4 v98, s[48:49]\n"
    "s_add_u32 m0, s62, 107520\n"
    "s_add_u32 s56, s58, 24576\n"
    "s_add_u32 s48, s66, s56\n"
    "s_addc_u32 s49, s67, 0\n"
    "global_load_lds_dwordx4 v98, s[48:49]\n"
    "ds_read_b128 v[176:179], v99 offset:49152\n"
    "ds_read_b128 v[180:183], v100 offset:49152\n"
    "ds_read_b128 v[184:187], v101 offset:49152\n"
    "ds_read_b128 v[188:191], v102 offset:49152\n"
    "s_waitcnt vmcnt(16) lgkmcnt(0)\n"
    "s_barrier\n"
    "s_add_u32 s57, s35, 7\n"
    "s_and_b32 s57, s57, 7\n"
    "s_lshl_b32 s56, s57, 7\n"
    "s_add_u32 s48, s64, s56\n"
    "s_addc_u32 s49, s65, 0\n"
    "s_add_u32 m0, s61, 49152\n"
    "s_nop 0\n"
    "global_load_lds_dwordx4 v96, s[48:49]\n"
    "s_add_u32 m0, s61, 50176\n"
    "s_nop 0\n"
    "global_load_lds_dwordx4 v97, s[48:49]\n"
    "s_add_u32 m0, s61, 51200\n"
    "s_add_u32 s48, s48, 16384\n"
    "s_addc_u32 s49, s49, 0\n"
    "global_load_lds_dwordx4 v96, s[48:49]\n"
    "s_add_u32 m0, s61, 52224\n"
    "s_nop 0\n"
    "global_load_lds_dwordx4 v97, s[48:49]\n"
    "s_mul_i32 s58, s57, 196608\n"
    "s_add_u32 m0, s62, 114688\n"
    "s_add_u32 s56, s58, 1024\n"
    "s_add_u32 s48, s66, s56\n"
    "s_addc_u32 s49, s67, 0\n"
    "global_load_lds_dwordx4 v98, s[48:49]\n"
    "s_add_u32 m0, s62, 115712\n"
    "s_add_u32 s56, s58, 25600\n"
    "s_add_u32 s48, s66, s56\n"
    "s_addc_u32 s49, s67, 0\n"
    "global_load_lds_dwordx4 v98, s[48:49]\n"
    "s_add_u32 m0, s62, 122880\n"
    "s_add_u32 s56, s58, 0\n"
    "s_add_u32 s48, s66, s56\n"
    "s_addc_u32 s49, s67, 0\n"
    "global_load_lds_dwordx4 v98, s[48:49]\n"
    "s_add_u32 m0, s62, 123904\n"
    "s_add_u32 s56, s58, 24576\n"
    "s_add_u32 s48, s66, s56\n"
    "s_addc_u32 s49, s67, 0\n"
    "global_load_lds_dwordx4 v98, s[48:49]\n"
    "ds_read_b128 v[192:195], v99 offset:0\n"
    "ds_read_b128 v[196:199], v100 offset:0\n"
    "ds_read_b128 v[200:203], v101 offset:0\n"
    "ds_read_b128 v[204:207], v102 offset:0\n"
    "v_dot2_f32_bf16 v0, v112, v120, 0\n"
    "v_dot2_f32_bf16 v1, v116, v120, 0\n"
    "v_dot2_f32_bf16 v0, v113, v121, v0\n"
    "v_dot2_f32_bf16 v1, v117, v121, v1\n"
    "v_dot2_f32_bf16 v0, v114, v122, v0\n"
    "v_dot2_f32_bf16 v1, v118, v122, v1\n"
    "v_dot2_f32_bf16 v0, v115, v123, v0\n"
    "v_dot2_f32_bf16 v1, v119, v123, v1\n"
    "s_nop 2\n"
    "v_permlane32_swap_b32 v0, v1\n"
    "v_add_f32 v0, v0, v1\n"
    "s_nop 1\n"
    "v_add_f32_dpp v0, v0, v0 row_ror:8 row_mask:0xf bank_mask:0xf\n"
    "s_nop 1\n"
    "v_add_f32_dpp v0, v0, v0 row_ror:4 row_mask:0xf bank_mask:0xf\n"
    "s_nop 1\n"
    "v_add_f32_dpp v0, v0, v0 row_ror:2 row_mask:0xf bank_mask:0xf\n"
    "s_nop 1\n"
    "v_add_f32_dpp v0, v0, v0 row_ror:1 row_mask:0xf bank_mask:0xf\n"
    "v_mov_b32 v1, v0\n"
    "s_nop 1\n"
    "v_permlane16_swap_b32 v0, v1\n"
    "v_add_f32 v0, v0, v1\n"
    "v_mul_f32 v0, 0xbfb8aa3b, v0\n"
    "v_exp_f32 v0, v0\n"
    "v_lshrrev_b32 v114, 3, v106\n"
    "v_add_f32 v0, 1.0, v0\n"
    "v_rcp_f32 v113, v0\n"
    "s_waitcnt vmcnt(16) lgkmcnt(0)\n"
    "s_barrier\n"
    "s_add_u32 s57, s35, 0\n"
    "s_and_b32 s57, s57, 7\n"
    "s_mul_i32 s58, s57, 196608\n"
    "s_add_u32 m0, s62, 131072\n"
    "s_add_u32 s56, s58, 0\n"
    "s_add_u32 s48, s68, s56\n"
    "s_addc_u32 s49, s69, 0\n"
    "global_load_lds_dwordx4 v98, s[48:49]\n"
    "s_add_u32 m0, s62, 132096\n"
    "s_add_u32 s56, s58, 24576\n"
    "s_add_u32 s48, s68, s56\n"
    "s_addc_u32 s49, s69, 0\n"
    "global_load_lds_dwordx4 v98, s[48:49]\n"
    "s_add_u32 s57, s35, 1\n"
    "s_and_b32 s57, s57, 7\n"
    "s_mul_i32 s58, s57, 196608\n"
    "s_add_u32 m0, s62, 139264\n"
    "s_add_u32 s56, s58, 0\n"
    "s_add_u32 s48, s68, s56\n"
    "s_addc_u32 s49, s69, 0\n"
    "global_load_lds_dwordx4 v98, s[48:49]\n"
    "s_add_u32 m0, s62, 140288\n"
    "s_add_u32 s56, s58, 24576\n"
    "s_add_u32 s48, s68, s56\n"
    "s_addc_u32 s49, s69, 0\n"
    "global_load_lds_dwordx4 v98, s[48:49]\n"
    "s_add_u32 s57, s35, 2\n"
    "s_and_b32 s57, s57, 7\n"
    "s_mul_i32 s58, s57, 196608\n"
    "s_add_u32 m0, s62, 147456\n"
    "s_add_u32 s56, s58, 0\n"
    "s_add_u32 s48, s68, s56\n"
    "s_addc_u32 s49, s69, 0\n"
    "global_load_lds_dwordx4 v98, s[48:49]\n"
    "s_add_u32 m0, s62, 148480\n"
    "s_add_u32 s56, s58, 24576\n"
    "s_add_u32 s48, s68, s56\n"
    "s_addc_u32 s49, s69, 0\n"
    "global_load_lds_dwordx4 v98, s[48:49]\n"
    "s_add_u32 s57, s35, 3\n"
    "s_and_b32 s57, s57, 7\n"
    "s_mul_i32 s58, s57, 196608\n"
    "s_add_u32 m0, s62, 0\n"
    "s_add_u32 s56, s58, 0\n"
    "s_add_u32 s48, s68, s56\n"
    "s_addc_u32 s49, s69, 0\n"
    "global_load_lds_dwordx4 v98, s[48:49]\n"
    "s_add_u32 m0, s62, 1024\n"
    "s_add_u32 s56, s58, 24576\n"
    "s_add_u32 s48, s68, s56\n"
    "s_addc_u32 s49, s69, 0\n"
    "global_load_lds_dwordx4 v98, s[48:49]\n"
    "ds_read_b128 v[208:211], v99 offset:16384\n"
    "ds_read_b128 v[212:215], v100 offset:16384\n"
    "ds_read_b128 v[216:219], v101 offset:16384\n"
    "ds_read_b128 v[220:223], v102 offset:16384\n"
    "s_waitcnt vmcnt(16) lgkmcnt(0)\n"
    "s_barrier\n"
    "s_add_u32 s57, s35, 4\n"
    "s_and_b32 s57, s57, 7\n"
    "s_mul_i32 s58, s57, 196608\n"
    "s_add_u32 m0, s62, 8192\n"
    "s_add_u32 s56, s58, 0\n"
    "s_add_u32 s48, s68, s56\n"
    "s_addc_u32 s49, s69, 0\n"
    "global_load_lds_dwordx4 v98, s[48:49]\n"
    "s_add_u32 m0, s62, 9216\n"
    "s_add_u32 s56, s58, 24576\n"
    "s_add_u32 s48, s68, s56\n"
    "s_addc_u32 s49, s69, 0\n"
    "global_load_lds_dwordx4 v98, s[48:49]\n"
    "s_add_u32 s57, s35, 5\n"
    "s_and_b32 s57, s57, 7\n"
    "s_mul_i32 s58, s57, 196608\n"
    "s_add_u32 m0, s62, 65536\n"
    "s_add_u32 s56, s58, 0\n"
    "s_add_u32 s48, s68, s56\n"
    "s_addc_u32 s49, s69, 0\n"
    "global_load_lds_dwordx4 v98, s[48:49]\n"
    "s_add_u32 m0, s62, 66560\n"
    "s_add_u32 s56, s58, 24576\n"
    "s_add_u32 s48, s68, s56\n"
    "s_addc_u32 s49, s69, 0\n"
    "global_load_lds_dwordx4 v98, s[48:49]\n"
    "s_add_u32 s57, s35, 6\n"
    "s_and_b32 s57, s57, 7\n"
    "s_mul_i32 s58, s57, 196608\n"
    "s_add_u32 m0, s62, 73728\n"
    "s_add_u32 s56, s58, 0\n"
    "s_add_u32 s48, s68, s56\n"
    "s_addc_u32 s49, s69, 0\n"
    "global_load_lds_dwordx4 v98, s[48:49]\n"
    "s_add_u32 m0, s62, 74752\n"
    "s_add_u32 s56, s58, 24576\n"
    "s_add_u32 s48, s68, s56\n"
    "s_addc_u32 s49, s69, 0\n"
    "global_load_lds_dwordx4 v98, s[48:49]\n"
    "s_add_u32 s57, s35, 7\n"
    "s_and_b32 s57, s57, 7\n"
    "s_mul_i32 s58, s57, 196608\n"
    "s_add_u32 m0, s62, 16384\n"
    "s_add_u32 s56, s58, 0\n"
    "s_add_u32 s48, s68, s56\n"
    "s_addc_u32 s49, s69, 0\n"
    "global_load_lds_dwordx4 v98, s[48:49]\n"
    "s_add_u32 m0, s62, 17408\n"
    "s_add_u32 s56, s58, 24576\n"
    "s_add_u32 s48, s68, s56\n"
    "s_addc_u32 s49, s69, 0\n"
    "global_load_lds_dwordx4 v98, s[48:49]\n"
    "ds_read_b128 v[224:227], v99 offset:32768\n"
    "ds_read_b128 v[228:231], v100 offset:32768\n"
    "ds_read_b128 v[232:235], v101 offset:32768\n"
    "ds_read_b128 v[236:239], v102 offset:32768\n"
    "s_waitcnt vmcnt(16) lgkmcnt(0)\n"
    "s_barrier\n"
    "ds_read_b128 v[240:243], v99 offset:49152\n"
    "ds_read_b128 v[244:247], v100 offset:49152\n"
    "ds_read_b128 v[248:251], v101 offset:49152\n"
    "ds_read_b128 v[252:255], v102 offset:49152\n"
    "s_waitcnt vmcnt(0) lgkmcnt(0)\n"
    "s_barrier\n"
    "ds_read_b64_tr_b16 v[64:65], v126 offset:0\n"
    "ds_read_b64_tr_b16 v[66:67], v126 offset:512\n"
    "ds_read_b64_tr_b16 v[68:69], v126 offset:256\n"
    "ds_read_b64_tr_b16 v[70:71], v126 offset:768\n"
    "ds_read_b64_tr_b16 v[72:73], v126 offset:2048\n"
    "ds_read_b64_tr_b16 v[74:75], v126 offset:2560\n"
    "ds_read_b64_tr_b16 v[76:77], v126 offset:2304\n"
    "ds_read_b64_tr_b16 v[78:79], v126 offset:2816\n"
    "ds_read_b64_tr_b16 v[80:81], v126 offset:4096\n"
    "ds_read_b64_tr_b16 v[82:83], v126 offset:4608\n"
    "ds_read_b64_tr_b16 v[84:85], v126 offset:4352\n"
    "ds_read_b64_tr_b16 v[86:87], v126 offset:4864\n"
    "s_waitcnt lgkmcnt(10)\n"
    "v_mfma_f32_32x32x16_bf16 v[0:15], v[64:67], v[128:131], 0\n"
    "ds_read_b64_tr_b16 v[88:89], v126 offset:6144\n"
    "ds_read_b64_tr_b16 v[90:91], v126 offset:6656\n"
    "s_waitcnt lgkmcnt(10)\n"
    "v_mfma_f32_32x32x16_bf16 v[16:31], v[68:71], v[128:131], 0\n"
    "ds_read_b64_tr_b16 v[92:93], v126 offset:6400\n"
    "ds_read_b64_tr_b16 v[94:95], v126 offset:6912\n"
    "s_waitcnt lgkmcnt(10)\n"
    "v_mfma_f32_32x32x16_bf16 v[0:15], v[72:75], v[132:135], v[0:15]\n"
    "ds_read_b64_tr_b16 v[64:65], v126 offset:8192\n"
    "ds_read_b64_tr_b16 v[66:67], v126 offset:8704\n"
    "s_waitcnt lgkmcnt(10)\n"
    "v_mfma_f32_32x32x16_bf16 v[16:31], v[76:79], v[132:135], v[16:31]\n"
    "ds_read_b64_tr_b16 v[68:69], v126 offset:8448\n"
    "ds_read_b64_tr_b16 v[70:71], v126 offset:8960\n"
    "s_waitcnt lgkmcnt(10)\n"
    "v_mfma_f32_32x32x16_bf16 v[0:15], v[80:83], v[136:139], v[0:15]\n"
    "ds_read_b64_tr_b16 v[72:73], v126 offset:10240\n"
    "ds_read_b64_tr_b16 v[74:75], v126 offset:10752\n"
    "s_waitcnt lgkmcnt(10)\n"
    "v_mfma_f32_32x32x16_bf16 v[16:31], v[84:87], v[136:139], v[16:31]\n"
    "ds_read_b64_tr_b16 v[76:77], v126 offset:10496\n"
    "ds_read_b64_tr_b16 v[78:79], v126 offset:11008\n"
    "s_waitcnt lgkmcnt(10)\n"
    "v_mfma_f32_32x32x16_bf16 v[0:15], v[88:91], v[140:143], v[0:15]\n"
    "ds_read_b64_tr_b16 v[80:81], v126 offset:12288\n"
    "ds_read_b64_tr_b16 v[82:83], v126 offset:12800\n"
    "s_waitcnt lgkmcnt(10)\n"
    "v_mfma_f32_32x32x16_bf16 v[16:31], v[92:95], v[140:143], v[16:31]\n"
    "ds_read_b64_tr_b16 v[84:85], v126 offset:12544\n"
    "ds_read_b64_tr_b16 v[86:87], v126 offset:13056\n"
    "s_waitcnt lgkmcnt(10)\n"
    "v_mfma_f32_32x32x16_bf16 v[0:15], v[64:67], v[144:147], v[0:15]\n"
    "ds_read_b64_tr_b16 v[88:89], v126 offset:14336\n"
    "ds_read_b64_tr_b16 v[90:91], v126 offset:14848\n"
    "s_waitcnt lgkmcnt(10)\n"
    "v_mfma_f32_32x32x16_bf16 v[16:31], v[68:71], v[144:147], v[16:31]\n"
    "ds_read_b64_tr_b16 v[92:93], v126 offset:14592\n"
    "ds_read_b64_tr_b16 v[94:95], v126 offset:15104\n"
    "s_waitcnt lgkmcnt(10)\n"
    "v_mfma_f32_32x32x16_bf16 v[0:15], v[72:75], v[148:151], v[0:15]\n"
    "ds_read_b64_tr_b16 v[64:65], v126 offset:16384\n"
    "ds_read_b64_tr_b16 v[66:67], v126 offset:16896\n"
    "s_waitcnt lgkmcnt(10)\n"
    "v_mfma_f32_32x32x16_bf16 v[16:31], v[76:79], v[148:151], v[16:31]\n"
    "ds_read_b64_tr_b16 v[68:69], v126 offset:16640\n"
    "ds_read_b64_tr_b16 v[70:71], v126 offset:17152\n"
    "s_waitcnt lgkmcnt(10)\n"
    "v_mfma_f32_32x32x16_bf16 v[0:15], v[80:83], v[152:155], v[0:15]\n"
    "ds_read_b64_tr_b16 v[72:73], v126 offset:18432\n"
    "ds_read_b64_tr_b16 v[74:75], v126 offset:18944\n"
    "s_waitcnt lgkmcnt(10)\n"
    "v_mfma_f32_32x32x16_bf16 v[16:31], v[84:87], v[152:155], v[16:31]\n"
    "ds_read_b64_tr_b16 v[76:77], v126 offset:18688\n"
    "ds_read_b64_tr_b16 v[78:79], v126 offset:19200\n"
    "s_waitcnt lgkmcnt(10)\n"
    "v_mfma_f32_32x32x16_bf16 v[0:15], v[88:91], v[156:159], v[0:15]\n"
    "ds_read_b64_tr_b16 v[80:81], v126 offset:20480\n"
    "ds_read_b64_tr_b16 v[82:83], v126 offset:20992\n"
    "s_waitcnt lgkmcnt(10)\n"
    "v_mfma_f32_32x32x16_bf16 v[16:31], v[92:95], v[156:159], v[16:31]\n"
    "ds_read_b64_tr_b16 v[84:85], v126 offset:20736\n"
    "ds_read_b64_tr_b16 v[86:87], v126 offset:21248\n"
    "s_waitcnt lgkmcnt(10)\n"
    "v_mfma_f32_32x32x16_bf16 v[0:15], v[64:67], v[160:163], v[0:15]\n"
    "ds_read_b64_tr_b16 v[88:89], v126 offset:22528\n"
    "ds_read_b64_tr_b16 v[90:91], v126 offset:23040\n"
    "s_waitcnt lgkmcnt(10)\n"
    "v_mfma_f32_32x32x16_bf16 v[16:31], v[68:71], v[160:163], v[16:31]\n"
    "ds_read_b64_tr_b16 v[92:93], v126 offset:22784\n"
    "ds_read_b64_tr_b16 v[94:95], v126 offset:23296\n"
    "s_waitcnt lgkmcnt(10)\n"
    "v_mfma_f32_32x32x16_bf16 v[0:15], v[72:75], v[164:167], v[0:15]\n"
    "ds_read_b64_tr_b16 v[64:65], v103 offset:0\n"
    "ds_read_b64_tr_b16 v[66:67], v103 offset:512\n"
    "s_waitcnt lgkmcnt(10)\n"
    "v_mfma_f32_32x32x16_bf16 v[16:31], v[76:79], v[164:167], v[16:31]\n"
    "ds_read_b64_tr_b16 v[68:69], v103 offset:256\n"
    "ds_read_b64_tr_b16 v[70:71], v103 offset:768\n"
    "s_waitcnt lgkmcnt(10)\n"
    "v_mfma_f32_32x32x16_bf16 v[0:15], v[80:83], v[168:171], v[0:15]\n"
    "ds_read_b64_tr_b16 v[72:73], v103 offset:2048\n"
    "ds_read_b64_tr_b16 v[74:75], v103 offset:2560\n"
    "s_waitcnt lgkmcnt(10)\n"
    "v_mfma_f32_32x32x16_bf16 v[16:31], v[84:87], v[168:171], v[16:31]\n"
    "ds_read_b64_tr_b16 v[76:77], v103 offset:2304\n"
    "ds_read_b64_tr_b16 v[78:79], v103 offset:2816\n"
    "s_waitcnt lgkmcnt(10)\n"
    "v_mfma_f32_32x32x16_bf16 v[0:15], v[88:91], v[172:175], v[0:15]\n"
    "ds_read_b64_tr_b16 v[80:81], v103 offset:4096\n"
    "ds_read_b64_tr_b16 v[82:83], v103 offset:4608\n"
    "s_waitcnt lgkmcnt(10)\n"
    "v_mfma_f32_32x32x16_bf16 v[16:31], v[92:95], v[172:175], v[16:31]\n"
    "ds_read_b64_tr_b16 v[84:85], v103 offset:4352\n"
    "ds_read_b64_tr_b16 v[86:87], v103 offset:4864\n"
    "s_waitcnt lgkmcnt(10)\n"
    "v_mfma_f32_32x32x16_bf16 v[0:15], v[64:67], v[176:179], v[0:15]\n"
    "ds_read_b64_tr_b16 v[88:89], v103 offset:6144\n"
    "ds_read_b64_tr_b16 v[90:91], v103 offset:6656\n"
    "s_waitcnt lgkmcnt(10)\n"
    "v_mfma_f32_32x32x16_bf16 v[16:31], v[68:71], v[176:179], v[16:31]\n"
    "ds_read_b64_tr_b16 v[92:93], v103 offset:6400\n"
    "ds_read_b64_tr_b16 v[94:95], v103 offset:6912\n"
    "s_waitcnt lgkmcnt(10)\n"
    "v_mfma_f32_32x32x16_bf16 v[0:15], v[72:75], v[180:183], v[0:15]\n"
    "ds_read_b64_tr_b16 v[64:65], v103 offset:8192\n"
    "ds_read_b64_tr_b16 v[66:67], v103 offset:8704\n"
    "s_waitcnt lgkmcnt(10)\n"
    "v_mfma_f32_32x32x16_bf16 v[16:31], v[76:79], v[180:183], v[16:31]\n"
    "ds_read_b64_tr_b16 v[68:69], v103 offset:8448\n"
    "ds_read_b64_tr_b16 v[70:71], v103 offset:8960\n"
    "s_waitcnt lgkmcnt(10)\n"
    "v_mfma_f32_32x32x16_bf16 v[0:15], v[80:83], v[184:187], v[0:15]\n"
    "ds_read_b64_tr_b16 v[72:73], v103 offset:10240\n"
    "ds_read_b64_tr_b16 v[74:75], v103 offset:10752\n"
    "s_waitcnt lgkmcnt(10)\n"
    "v_mfma_f32_32x32x16_bf16 v[16:31], v[84:87], v[184:187], v[16:31]\n"
    "ds_read_b64_tr_b16 v[76:77], v103 offset:10496\n"
    "ds_read_b64_tr_b16 v[78:79], v103 offset:11008\n"
    "s_waitcnt lgkmcnt(10)\n"
    "v_mfma_f32_32x32x16_bf16 v[0:15], v[88:91], v[188:191], v[0:15]\n"
    "ds_read_b64_tr_b16 v[80:81], v103 offset:12288\n"
    "ds_read_b64_tr_b16 v[82:83], v103 offset:12800\n"
    "s_waitcnt lgkmcnt(10)\n"
    "v_mfma_f32_32x32x16_bf16 v[16:31], v[92:95], v[188:191], v[16:31]\n"
    "ds_read_b64_tr_b16 v[84:85], v103 offset:12544\n"
    "ds_read_b64_tr_b16 v[86:87], v103 offset:13056\n"
    "s_waitcnt lgkmcnt(10)\n"
    "v_mfma_f32_32x32x16_bf16 v[0:15], v[64:67], v[192:195], v[0:15]\n"
    "ds_read_b64_tr_b16 v[88:89], v103 offset:14336\n"
    "ds_read_b64_tr_b16 v[90:91], v103 offset:14848\n"
    "s_waitcnt lgkmcnt(10)\n"
    "v_mfma_f32_32x32x16_bf16 v[16:31], v[68:71], v[192:195], v[16:31]\n"
    "ds_read_b64_tr_b16 v[92:93], v103 offset:14592\n"
    "ds_read_b64_tr_b16 v[94:95], v103 offset:15104\n"
    "s_waitcnt lgkmcnt(10)\n"
    "v_mfma_f32_32x32x16_bf16 v[0:15], v[72:75], v[196:199], v[0:15]\n"
    "ds_read_b64_tr_b16 v[64:65], v125 offset:0\n"
    "ds_read_b64_tr_b16 v[66:67], v125 offset:512\n"
    "s_waitcnt lgkmcnt(10)\n"
    "v_mfma_f32_32x32x16_bf16 v[16:31], v[76:79], v[196:199], v[16:31]\n"
    "ds_read_b64_tr_b16 v[68:69], v125 offset:256\n"
    "ds_read_b64_tr_b16 v[70:71], v125 offset:768\n"
    "s_waitcnt lgkmcnt(10)\n"
    "v_mfma_f32_32x32x16_bf16 v[0:15], v[80:83], v[200:203], v[0:15]\n"
    "ds_read_b64_tr_b16 v[72:73], v125 offset:2048\n"
    "ds_read_b64_tr_b16 v[74:75], v125 offset:2560\n"
    "s_waitcnt lgkmcnt(10)\n"
    "v_mfma_f32_32x32x16_bf16 v[16:31], v[84:87], v[200:203], v[16:31]\n"
    "ds_read_b64_tr_b16 v[76:77], v125 offset:2304\n"
    "ds_read_b64_tr_b16 v[78:79], v125 offset:2816\n"
    "s_waitcnt lgkmcnt(10)\n"
    "v_mfma_f32_32x32x16_bf16 v[0:15], v[88:91], v[204:207], v[0:15]\n"
    "ds_read_b64_tr_b16 v[80:81], v125 offset:4096\n"
    "ds_read_b64_tr_b16 v[82:83], v125 offset:4608\n"
    "s_waitcnt lgkmcnt(10)\n"
    "v_mfma_f32_32x32x16_bf16 v[16:31], v[92:95], v[204:207], v[16:31]\n"
    "ds_read_b64_tr_b16 v[84:85], v125 offset:4352\n"
    "ds_read_b64_tr_b16 v[86:87], v125 offset:4864\n"
    "s_waitcnt lgkmcnt(10)\n"
    "v_mfma_f32_32x32x16_bf16 v[0:15], v[64:67], v[208:211], v[0:15]\n"
    "ds_read_b64_tr_b16 v[88:89], v125 offset:6144\n"
    "ds_read_b64_tr_b16 v[90:91], v125 offset:6656\n"
    "s_waitcnt lgkmcnt(10)\n"
    "v_mfma_f32_32x32x16_bf16 v[16:31], v[68:71], v[208:211], v[16:31]\n"
    "ds_read_b64_tr_b16 v[92:93], v125 offset:6400\n"
    "ds_read_b64_tr_b16 v[94:95], v125 offset:6912\n"
    "s_waitcnt lgkmcnt(10)\n"
    "v_mfma_f32_32x32x16_bf16 v[0:15], v[72:75], v[212:215], v[0:15]\n"
    "ds_read_b64_tr_b16 v[64:65], v125 offset:8192\n"
    "ds_read_b64_tr_b16 v[66:67], v125 offset:8704\n"
    "s_waitcnt lgkmcnt(10)\n"
    "v_mfma_f32_32x32x16_bf16 v[16:31], v[76:79], v[212:215], v[16:31]\n"
    "ds_read_b64_tr_b16 v[68:69], v125 offset:8448\n"
    "ds_read_b64_tr_b16 v[70:71], v125 offset:8960\n"
    "s_waitcnt lgkmcnt(10)\n"
    "v_mfma_f32_32x32x16_bf16 v[0:15], v[80:83], v[216:219], v[0:15]\n"
    "ds_read_b64_tr_b16 v[72:73], v125 offset:10240\n"
    "ds_read_b64_tr_b16 v[74:75], v125 offset:10752\n"
    "s_waitcnt lgkmcnt(10)\n"
    "v_mfma_f32_32x32x16_bf16 v[16:31], v[84:87], v[216:219], v[16:31]\n"
    "ds_read_b64_tr_b16 v[76:77], v125 offset:10496\n"
    "ds_read_b64_tr_b16 v[78:79], v125 offset:11008\n"
    "s_waitcnt lgkmcnt(10)\n"
    "v_mfma_f32_32x32x16_bf16 v[0:15], v[88:91], v[220:223], v[0:15]\n"
    "ds_read_b64_tr_b16 v[80:81], v125 offset:12288\n"
    "ds_read_b64_tr_b16 v[82:83], v125 offset:12800\n"
    "s_waitcnt lgkmcnt(10)\n"
    "v_mfma_f32_32x32x16_bf16 v[16:31], v[92:95], v[220:223], v[16:31]\n"
    "ds_read_b64_tr_b16 v[84:85], v125 offset:12544\n"
    "ds_read_b64_tr_b16 v[86:87], v125 offset:13056\n"
    "s_waitcnt lgkmcnt(10)\n"
    "v_mfma_f32_32x32x16_bf16 v[0:15], v[64:67], v[224:227], v[0:15]\n"
    "ds_read_b64_tr_b16 v[88:89], v125 offset:14336\n"
    "ds_read_b64_tr_b16 v[90:91], v125 offset:14848\n"
    "s_waitcnt lgkmcnt(10)\n"
    "v_mfma_f32_32x32x16_bf16 v[16:31], v[68:71], v[224:227], v[16:31]\n"
    "ds_read_b64_tr_b16 v[92:93], v125 offset:14592\n"
    "ds_read_b64_tr_b16 v[94:95], v125 offset:15104\n"
    "s_waitcnt lgkmcnt(10)\n"
    "v_mfma_f32_32x32x16_bf16 v[0:15], v[72:75], v[228:231], v[0:15]\n"
    "ds_read_b64_tr_b16 v[64:65], v103 offset:16384\n"
    "ds_read_b64_tr_b16 v[66:67], v103 offset:16896\n"
    "s_waitcnt lgkmcnt(10)\n"
    "v_mfma_f32_32x32x16_bf16 v[16:31], v[76:79], v[228:231], v[16:31]\n"
    "ds_read_b64_tr_b16 v[68:69], v103 offset:16640\n"
    "ds_read_b64_tr_b16 v[70:71], v103 offset:17152\n"
    "s_waitcnt lgkmcnt(10)\n"
    "v_mfma_f32_32x32x16_bf16 v[0:15], v[80:83], v[232:235], v[0:15]\n"
    "ds_read_b64_tr_b16 v[72:73], v103 offset:18432\n"
    "ds_read_b64_tr_b16 v[74:75], v103 offset:18944\n"
    "s_waitcnt lgkmcnt(10)\n"
    "v_mfma_f32_32x32x16_bf16 v[16:31], v[84:87], v[232:235], v[16:31]\n"
    "ds_read_b64_tr_b16 v[76:77], v103 offset:18688\n"
    "ds_read_b64_tr_b16 v[78:79], v103 offset:19200\n"
    "s_waitcnt lgkmcnt(10)\n"
    "v_mfma_f32_32x32x16_bf16 v[0:15], v[88:91], v[236:239], v[0:15]\n"
    "ds_read_b64_tr_b16 v[80:81], v103 offset:20480\n"
    "ds_read_b64_tr_b16 v[82:83], v103 offset:20992\n"
    "s_waitcnt lgkmcnt(10)\n"
    "v_mfma_f32_32x32x16_bf16 v[16:31], v[92:95], v[236:239], v[16:31]\n"
    "ds_read_b64_tr_b16 v[84:85], v103 offset:20736\n"
    "ds_read_b64_tr_b16 v[86:87], v103 offset:21248\n"
    "s_waitcnt lgkmcnt(10)\n"
    "v_mfma_f32_32x32x16_bf16 v[0:15], v[64:67], v[240:243], v[0:15]\n"
    "ds_read_b64_tr_b16 v[88:89], v103 offset:22528\n"
    "ds_read_b64_tr_b16 v[90:91], v103 offset:23040\n"
    "s_waitcnt lgkmcnt(10)\n"
    "v_mfma_f32_32x32x16_bf16 v[16:31], v[68:71], v[240:243], v[16:31]\n"
    "ds_read_b64_tr_b16 v[92:93], v103 offset:22784\n"
    "ds_read_b64_tr_b16 v[94:95], v103 offset:23296\n"
    "s_waitcnt lgkmcnt(10)\n"
    "v_mfma_f32_32x32x16_bf16 v[0:15], v[72:75], v[244:247], v[0:15]\n"
    "s_waitcnt lgkmcnt(8)\n"
    "v_mfma_f32_32x32x16_bf16 v[16:31], v[76:79], v[244:247], v[16:31]\n"
    "s_waitcnt lgkmcnt(6)\n"
    "v_mfma_f32_32x32x16_bf16 v[0:15], v[80:83], v[248:251], v[0:15]\n"
    "s_waitcnt lgkmcnt(4)\n"
    "v_mfma_f32_32x32x16_bf16 v[16:31], v[84:87], v[248:251], v[16:31]\n"
    "s_waitcnt lgkmcnt(2)\n"
    "v_mfma_f32_32x32x16_bf16 v[0:15], v[88:91], v[252:255], v[0:15]\n"
    "s_waitcnt lgkmcnt(0)\n"
    "v_mfma_f32_32x32x16_bf16 v[16:31], v[92:95], v[252:255], v[16:31]\n"
    "22: s_cbranch_execz 22f\n"
    "s_setprio 3\n"
    "v_readfirstlane_b32 s39, v104\n"
    "s_mov_b64 exec, 1\n"
    "ds_add_u32 v108, v109 offset:16\n"
    "s_mov_b64 exec, -1\n"
    "s_add_u32 s82, s39, 1\n"
    "s_xor_b32 s83, s82, 1540483477\n"
    "s_mov_b32 s54, 0x3fb8aa3b\n"
    "s_mov_b32 s55, 0x3fb8aa3b\n"
    "s_mov_b32 s58, 0x5d800000\n"
    "s_mov_b64 s[52:53], 0\n"
    "ds_read_b64_tr_b16 v[64:65], v124 offset:0\n"
    "ds_read_b64_tr_b16 v[66:67], v124 offset:256\n"
    "ds_read_b64_tr_b16 v[68:69], v124 offset:512\n"
    "ds_read_b64_tr_b16 v[70:71], v124 offset:768\n"
    "ds_read_b64_tr_b16 v[72:73], v124 offset:1024\n"
    "ds_read_b64_tr_b16 v[74:75], v124 offset:1280\n"
    "ds_read_b64_tr_b16 v[76:77], v124 offset:1536\n"
    "ds_read_b64_tr_b16 v[78:79], v124 offset:1792\n"
    "v_pk_mul_f32 v[0:1], v[0:1], s[54:55]\n"
    "v_pk_mul_f32 v[2:3], v[2:3], s[54:55]\n"
    "v_pk_mul_f32 v[4:5], v[4:5], s[54:55]\n"
    "v_pk_mul_f32 v[6:7], v[6:7], s[54:55]\n"
    "v_cvt_pk_bf16_f32 v80, v0, v1\n"
    "v_cvt_pk_bf16_f32 v81, v2, v3\n"
    "v_cvt_pk_bf16_f32 v82, v4, v5\n"
    "v_cvt_pk_bf16_f32 v83, v6, v7\n"
    "v_pk_mul_f32 v[8:9], v[8:9], s[54:55]\n"
    "v_pk_mul_f32 v[10:11], v[10:11], s[54:55]\n"
    "23: s_cbranch_execz 23f\n"
    "v_pk_mul_f32 v[12:13], v[12:13], s[54:55]\n"
    "v_pk_mul_f32 v[14:15], v[14:15], s[54:55]\n"
    "v_cvt_pk_bf16_f32 v84, v8, v9\n"
    "v_cvt_pk_bf16_f32 v85, v10, v11\n"
    "v_cvt_pk_bf16_f32 v86, v12, v13\n"
    "v_cvt_pk_bf16_f32 v87, v14, v15\n"
    "s_waitcnt lgkmcnt(0)\n"
    "v_mfma_f32_32x32x16_bf16 v[128:143], v[64:67], v[80:83], 0\n"
    "v_pk_mul_f32 v[16:17], v[16:17], s[54:55]\n"
    "v_pk_mul_f32 v[18:19], v[18:19], s[54:55]\n"
    "v_pk_mul_f32 v[20:21], v[20:21], s[54:55]\n"
    "v_pk_mul_f32 v[22:23], v[22:23], s[54:55]\n"
    "v_cvt_pk_bf16_f32 v88, v16, v17\n"
    "v_cvt_pk_bf16_f32 v89, v18, v19\n"
    "v_cvt_pk_bf16_f32 v90, v20, v21\n"
    "v_cvt_pk_bf16_f32 v91, v22, v23\n"
    "v_mfma_f32_32x32x16_bf16 v[144:159], v[68:71], v[84:87], 0\n"
    "v_pk_mul_f32 v[24:25], v[24:25], s[54:55]\n"
    "v_pk_mul_f32 v[26:27], v[26:27], s[54:55]\n"
    "v_pk_mul_f32 v[28:29], v[28:29], s[54:55]\n"
    "v_pk_mul_f32 v[30:31], v[30:31], s[54:55]\n"
    "v_cvt_pk_bf16_f32 v92, v24, v25\n"
    "v_cvt_pk_bf16_f32 v93, v26, v27\n"
    "v_cvt_pk_bf16_f32 v94, v28, v29\n"
    "v_cvt_pk_bf16_f32 v95, v30, v31\n"
    "v_mfma_f32_32x32x16_bf16 v[160:175], v[72:75], v[88:91], 0\n"
    "s_nop 0\n"
    "24: s_cbranch_execz 24f\n"
    "v_mfma_f32_32x32x16_bf16 v[176:191], v[76:79], v[92:95], 0\n"
    "v_exp_f32 v136, -v128\n"
    "v_exp_f32 v152, -v144\n"
    "s_nop 7\n"
    "v_exp_f32 v168, -v160\n"
    "s_nop 0\n"
    "v_exp_f32 v184, -v176\n"
    "v_exp_f32 v137, -v129\n"
    "v_exp_f32 v153, -v145\n"
    "v_exp_f32 v169, -v161\n"
    "v_exp_f32 v185, -v177\n"
    "v_exp_f32 v138, -v130\n"
    "v_exp_f32 v154, -v146\n"
    "v_exp_f32 v170, -v162\n"
    "v_exp_f32 v186, -v178\n"
    "v_exp_f32 v139, -v131\n"
    "v_exp_f32 v155, -v147\n"
    "v_exp_f32 v171, -v163\n"
    "v_exp_f32 v187, -v179\n"
    "v_exp_f32 v140, -v132\n"
    "v_exp_f32 v156, -v148\n"
    "v_exp_f32 v172, -v164\n"
    "v_exp_f32 v188, -v180\n"
    "v_exp_f32 v141, -v133\n"
    "v_exp_f32 v157, -v149\n"
    "v_exp_f32 v173, -v165\n"
    "v_exp_f32 v189, -v181\n"
    "v_exp_f32 v142, -v134\n"
    "v_exp_f32 v158, -v150\n"
    "v_exp_f32 v174, -v166\n"
    "v_exp_f32 v190, -v182\n"
    "v_exp_f32 v143, -v135\n"
    "v_exp_f32 v159, -v151\n"
    "v_exp_f32 v175, -v167\n"
    "v_exp_f32 v191, -v183\n"
    "v_exp_f32 v128, v128\n"
    "v_exp_f32 v144, v144\n"
    "v_exp_f32 v160, v160\n"
    "v_exp_f32 v176, v176\n"
    "v_exp_f32 v129, v129\n"
    "v_exp_f32 v145, v145\n"
    "v_exp_f32 v161, v161\n"
    "v_exp_f32 v177, v177\n"
    "v_exp_f32 v130, v130\n"
    "v_exp_f32 v146, v146\n"
    "v_exp_f32 v162, v162\n"
    "v_exp_f32 v178, v178\n"
    "v_exp_f32 v131, v131\n"
    "v_exp_f32 v147, v147\n"
    "v_exp_f32 v163, v163\n"
    "v_exp_f32 v179, v179\n"
    "25: s_cbranch_execz 25f\n"
    "v_exp_f32 v132, v132\n"
    "v_exp_f32 v148, v148\n"
    "v_exp_f32 v164, v164\n"
    "v_exp_f32 v180, v180\n"
    "v_exp_f32 v133, v133\n"
    "v_exp_f32 v149, v149\n"
    "v_exp_f32 v165, v165\n"
    "v_exp_f32 v181, v181\n"
    "v_exp_f32 v134, v134\n"
    "v_exp_f32 v150, v150\n"
    "v_exp_f32 v166, v166\n"
    "v_exp_f32 v182, v182\n"
    "v_exp_f32 v135, v135\n"
    "v_exp_f32 v151, v151\n"
    "v_exp_f32 v167, v167\n"
    "v_exp_f32 v183, v183\n"
    "v_pk_add_f32 v[194:195], v[136:137], v[138:139]\n"
    "v_pk_add_f32 v[198:199], v[152:153], v[154:155]\n"
    "v_pk_add_f32 v[202:203], v[168:169], v[170:171]\n"
    "v_pk_add_f32 v[206:207], v[184:185], v[186:187]\n"
    "v_pk_add_f32 v[192:193], v[128:129], v[130:131]\n"
    "v_pk_add_f32 v[196:197], v[144:145], v[146:147]\n"
    "v_pk_add_f32 v[200:201], v[160:161], v[162:163]\n"
    "v_pk_add_f32 v[204:205], v[176:177], v[178:179]\n"
    "v_pk_add_f32 v[194:195], v[194:195], v[140:141]\n"
    "v_pk_add_f32 v[198:199], v[198:199], v[156:157]\n"
    "v_pk_add_f32 v[202:203], v[202:203], v[172:173]\n"
    "v_pk_add_f32 v[206:207], v[206:207], v[188:189]\n"
    "v_pk_add_f32 v[192:193], v[192:193], v[132:133]\n"
    "v_pk_add_f32 v[196:197], v[196:197], v[148:149]\n"
    "v_pk_add_f32 v[200:201], v[200:201], v[164:165]\n"
    "v_pk_add_f32 v[204:205], v[204:205], v[180:181]\n"
    "v_pk_add_f32 v[194:195], v[194:195], v[142:143]\n"
    "v_pk_add_f32 v[198:199], v[198:199], v[158:159]\n"
    "22: s_cbranch_execz 22f\n"
    "v_pk_add_f32 v[202:203], v[202:203], v[174:175]\n"
    "v_pk_add_f32 v[206:207], v[206:207], v[190:191]\n"
    "v_pk_add_f32 v[192:193], v[192:193], v[134:135]\n"
    "v_pk_add_f32 v[196:197], v[196:197], v[150:151]\n"
    "v_pk_add_f32 v[200:201], v[200:201], v[166:167]\n"
    "v_pk_add_f32 v[204:205], v[204:205], v[182:183]\n"
    "v_add_f32 v192, v192, v193\n"
    "v_add_f32 v196, v196, v197\n"
    "v_add_f32 v200, v200, v201\n"
    "v_add_f32 v204, v204, v205\n"
    "v_add_f32 v193, v194, v195\n"
    "v_add_f32 v197, v198, v199\n"
    "v_add_f32 v201, v202, v203\n"
    "v_add_f32 v205, v206, v207\n"
    "v_permlane32_swap_b32 v192, v193\n"
    "v_permlane32_swap_b32 v196, v197\n"
    "v_permlane32_swap_b32 v200, v201\n"
    "v_permlane32_swap_b32 v204, v205\n"
    "v_add_f32 v192, v192, v193\n"
    "v_add_f32 v196, v196, v197\n"
    "v_add_f32 v200, v200, v201\n"
    "v_add_f32 v204, v204, v205\n"
    "v_cmp_nge_f32 vcc, s58, v192\n"
    "s_or_b64 s[52:53], s[52:53], vcc\n"
    "v_cmp_nge_f32 vcc, s58, v196\n"
    "s_or_b64 s[52:53], s[52:53], vcc\n"
    "v_cmp_nge_f32 vcc, s58, v200\n"
    "s_or_b64 s[52:53], s[52:53], vcc\n"
    "v_cmp_nge_f32 vcc, s58, v204\n"
    "s_or_b64 s[52:53], s[52:53], vcc\n"
    "v_rcp_f32 v192, v192\n"
    "v_rcp_f32 v196, v196\n"
    "v_rcp_f32 v200, v200\n"
    "v_rcp_f32 v204, v204\n"
    "v_mov_b32 v193, v192\n"
    "v_mov_b32 v197, v196\n"
    "v_mov_b32 v201, v200\n"
    "v_mov_b32 v205, v204\n"
    "23: s_cbranch_execz 23f\n"
    "v_permlane32_swap_b32 v192, v193\n"
    "v_permlane32_swap_b32 v196, v197\n"
    "v_permlane32_swap_b32 v200, v201\n"
    "v_permlane32_swap_b32 v204, v205\n"
    "v_pk_mul_f32 v[128:129], v[128:129], v[192:193] op_sel_hi:[1,0]\n"
    "v_pk_mul_f32 v[144:145], v[144:145], v[196:197] op_sel_hi:[1,0]\n"
    "v_pk_mul_f32 v[160:161], v[160:161], v[200:201] op_sel_hi:[1,0]\n"
    "v_pk_mul_f32 v[176:177], v[176:177], v[204:205] op_sel_hi:[1,0]\n"
    "v_pk_mul_f32 v[130:131], v[130:131], v[192:193] op_sel_hi:[1,0]\n"
    "v_pk_mul_f32 v[146:147], v[146:147], v[196:197] op_sel_hi:[1,0]\n"
    "v_pk_mul_f32 v[162:163], v[162:163], v[200:201] op_sel_hi:[1,0]\n"
    "v_pk_mul_f32 v[178:179], v[178:179], v[204:205] op_sel_hi:[1,0]\n"
    "v_pk_mul_f32 v[132:133], v[132:133], v[192:193] op_sel_hi:[1,0]\n"
    "v_pk_mul_f32 v[148:149], v[148:149], v[196:197] op_sel_hi:[1,0]\n"
    "v_pk_mul_f32 v[164:165], v[164:165], v[200:201] op_sel_hi:[1,0]\n"
    "v_pk_mul_f32 v[180:181], v[180:181], v[204:205] op_sel_hi:[1,0]\n"
    "v_pk_mul_f32 v[134:135], v[134:135], v[192:193] op_sel_hi:[1,0]\n"
    "v_pk_mul_f32 v[150:151], v[150:151], v[196:197] op_sel_hi:[1,0]\n"
    "v_pk_mul_f32 v[166:167], v[166:167], v[200:201] op_sel_hi:[1,0]\n"
    "v_pk_mul_f32 v[182:183], v[182:183], v[204:205] op_sel_hi:[1,0]\n"
    "v_pk_mul_f32 v[136:137], v[136:137], v[192:193] op_sel:[0,1] op_sel_hi:[1,1]\n"
    "v_pk_mul_f32 v[152:153], v[152:153], v[196:197] op_sel:[0,1] op_sel_hi:[1,1]\n"
    "v_pk_mul_f32 v[168:169], v[168:169], v[200:201] op_sel:[0,1] op_sel_hi:[1,1]\n"
    "v_pk_mul_f32 v[184:185], v[184:185], v[204:205] op_sel:[0,1] op_sel_hi:[1,1]\n"
    "v_pk_mul_f32 v[138:139], v[138:139], v[192:193] op_sel:[0,1] op_sel_hi:[1,1]\n"
    "v_pk_mul_f32 v[154:155], v[154:155], v[196:197] op_sel:[0,1] op_sel_hi:[1,1]\n"
    "24: s_cbranch_execz 24f\n"
    "v_pk_mul_f32 v[170:171], v[170:171], v[200:201] op_sel:[0,1] op_sel_hi:[1,1]\n"
    "v_pk_mul_f32 v[186:187], v[186:187], v[204:205] op_sel:[0,1] op_sel_hi:[1,1]\n"
    "v_pk_mul_f32 v[140:141], v[140:141], v[192:193] op_sel:[0,1] op_sel_hi:[1,1]\n"
    "v_pk_mul_f32 v[156:157], v[156:157], v[196:197] op_sel:[0,1] op_sel_hi:[1,1]\n"
    "v_pk_mul_f32 v[172:173], v[172:173], v[200:201] op_sel:[0,1] op_sel_hi:[1,1]\n"
    "v_pk_mul_f32 v[188:189], v[188:189], v[204:205] op_sel:[0,1] op_sel_hi:[1,1]\n"
    "v_pk_mul_f32 v[142:143], v[142:143], v[192:193] op_sel:[0,1] op_sel_hi:[1,1]\n"
    "v_pk_mul_f32 v[158:159], v[158:159], v[196:197] op_sel:[0,1] op_sel_hi:[1,1]\n"
    "v_pk_mul_f32 v[174:175], v[174:175], v[200:201] op_sel:[0,1] op_sel_hi:[1,1]\n"
    "v_pk_mul_f32 v[190:191], v[190:191], v[204:205] op_sel:[0,1] op_sel_hi:[1,1]\n"
    "v_cvt_pk_bf16_f32 v128, v128, v129\n"
    "v_cvt_pk_bf16_f32 v144, v144, v145\n"
    "v_cvt_pk_bf16_f32 v160, v160, v161\n"
    "v_cvt_pk_bf16_f32 v176, v176, v177\n"
    "v_cvt_pk_bf16_f32 v129, v130, v131\n"
    "v_cvt_pk_bf16_f32 v145, v146, v147\n"
    "v_cvt_pk_bf16_f32 v161, v162, v163\n"
    "v_cvt_pk_bf16_f32 v177, v178, v179\n"
    "v_cvt_pk_bf16_f32 v130, v132, v133\n"
    "v_cvt_pk_bf16_f32 v146, v148, v149\n"
    "v_cvt_pk_bf16_f32 v162, v164, v165\n"
    "v_cvt_pk_bf16_f32 v178, v180, v181\n"
    "v_cvt_pk_bf16_f32 v131, v134, v135\n"
    "v_cvt_pk_bf16_f32 v147, v150, v151\n"
    "v_cvt_pk_bf16_f32 v163, v166, v167\n"
    "v_cvt_pk_bf16_f32 v179, v182, v183\n"
    "25: s_cbranch_execz 25f\n"
    "v_cvt_pk_bf16_f32 v132, v136, v137\n"
    "v_cvt_pk_bf16_f32 v148, v152, v153\n"
    "v_cvt_pk_bf16_f32 v164, v168, v169\n"
    "v_cvt_pk_bf16_f32 v180, v184, v185\n"
    "v_cvt_pk_bf16_f32 v133, v138, v139\n"
    "v_cvt_pk_bf16_f32 v149, v154, v155\n"
    "v_cvt_pk_bf16_f32 v165, v170, v171\n"
    "v_cvt_pk_bf16_f32 v181, v186, v187\n"
    "v_cvt_pk_bf16_f32 v134, v140, v141\n"
    "v_cvt_pk_bf16_f32 v150, v156, v157\n"
    "v_cvt_pk_bf16_f32 v166, v172, v173\n"
    "v_cvt_pk_bf16_f32 v182, v188, v189\n"
    "v_cvt_pk_bf16_f32 v135, v142, v143\n"
    "v_cvt_pk_bf16_f32 v151, v158, v159\n"
    "v_cvt_pk_bf16_f32 v167, v174, v175\n"
    "v_cvt_pk_bf16_f32 v183, v190, v191\n"
    "s_cmp_eq_u64 s[52:53], 0\n"
    "s_cbranch_scc1 J3\n"
    "s_getpc_b64 s[52:53]\n"
    "s_add_u32 s52, s52, 12\n"
    "s_addc_u32 s53, s53, 0\n"
    "s_branch PE_\n"
    "J3:\n"
    "v_mov_b64 v[0:1], 0\n"
    "v_mov_b64 v[2:3], 0\n"
    "v_mov_b64 v[4:5], 0\n"
    "v_mov_b64 v[6:7], 0\n"
    "v_mov_b64 v[8:9], 0\n"
    "v_mov_b64 v[10:11], 0\n"
    "v_mov_b64 v[12:13], 0\n"
    "22: s_cbranch_execz 22f\n"
    "v_mov_b64 v[14:15], 0\n"
    "v_mov_b64 v[16:17], 0\n"
    "v_mov_b64 v[18:19], 0\n"
    "v_mov_b64 v[20:21], 0\n"
    "v_mov_b64 v[22:23], 0\n"
    "v_mov_b64 v[24:25], 0\n"
    "v_mov_b64 v[26:27], 0\n"
    "v_mov_b64 v[28:29], 0\n"
    "v_mov_b64 v[30:31], 0\n"
    "v_mov_b64 v[88:89], 0\n"
    "v_mov_b64 v[90:91], 0\n"
    "v_mov_b64 v[92:93], 0\n"
    "v_mov_b64 v[94:95], 0\n"
    "v_and_b32 v192, 27, v106\n"
    "v_cmp_eq_u32 vcc, 16, v192\n"
    "v_mov_b32 v193, 0x3f803f80\n"
    "v_mov_b32 v250, 0xdddddddd\n"
    "v_cndmask_b32 v196, 0, v193, vcc\n"
    "v_mov_b32 v197, v196\n"
    "v_mov_b64 v[198:199], v[196:197]\n"
    "v_mov_b64 v[200:201], v[196:197]\n"
    "v_mov_b64 v[202:203], v[196:197]\n"
    "v_mov_b64 v[204:205], v[196:197]\n"
    "v_mov_b64 v[206:207], v[196:197]\n"
    "s_lshl_b32 s56, s29, 16\n"
    "v_add_u32 v104, s56, v105\n"
    "s_branch B_\n"
    "B1_:\n"
    "s_lshl_b32 s56, s38, 15\n"
    "s_add_u32 s64, s8, s56\n"
    "s_addc_u32 s65, s9, 0\n"
    "s_mul_i32 s56, s28, 49152\n"
    "s_lshl_b32 s57, s32, 7\n"
    "s_add_u32 s56, s56, s57\n"
    "s_add_u32 s57, s56, 2048\n"
    "s_add_u32 s66, s10, s57\n"
    "s_addc_u32 s67, s11, 0\n"
    "s_add_u32 s57, s56, 1024\n"
    "s_add_u32 s68, s14, s57\n"
    "s_addc_u32 s69, s15, 0\n"
    "s_lshl_b32 s61, s28, 12\n"
    "s_lshl_b32 s62, s28, 11\n"
    "v_add_u32 v125, 0x10000, v103\n"
    "v_add_u32 v126, 0x20000, v103\n"
    "s_add_u32 s57, s35, 2\n"
    "s_and_b32 s57, s57, 7\n"
    "s_lshl_b32 s56, s57, 7\n"
    "s_add_u32 s48, s64, s56\n"
    "s_addc_u32 s49, s65, 0\n"
    "s_add_u32 m0, s61, 32768\n"
    "s_nop 0\n"
    "global_load_lds_dwordx4 v96, s[48:49]\n"
    "s_add_u32 m0, s61, 33792\n"
    "s_nop 0\n"
    "global_load_lds_dwordx4 v97, s[48:49]\n"
    "s_add_u32 m0, s61, 34816\n"
    "s_add_u32 s48, s48, 16384\n"
    "s_addc_u32 s49, s49, 0\n"
    "global_load_lds_dwordx4 v96, s[48:49]\n"
    "s_add_u32 m0, s61, 35840\n"
    "s_nop 0\n"
    "global_load_lds_dwordx4 v97, s[48:49]\n"
    "s_mul_i32 s58, s57, 196608\n"
    "s_add_u32 m0, s62, 81920\n"
    "s_add_u32 s56, s58, 0\n"
    "s_add_u32 s48, s66, s56\n"
    "s_addc_u32 s49, s67, 0\n"
    "global_load_lds_dwordx4 v98, s[48:49]\n"
    "s_add_u32 m0, s62, 82944\n"
    "s_add_u32 s56, s58, 24576\n"
    "s_add_u32 s48, s66, s56\n"
    "s_addc_u32 s49, s67, 0\n"
    "global_load_lds_dwordx4 v98, s[48:49]\n"
    "s_waitcnt vmcnt(10) lgkmcnt(0)\n"
    "s_barrier\n"
    "s_add_u32 s57, s35, 3\n"
    "s_and_b32 s57, s57, 7\n"
    "s_lshl_b32 s56, s57, 7\n"
    "s_add_u32 s48, s64, s56\n"
    "s_addc_u32 s49, s65, 0\n"
    "s_add_u32 m0, s61, 49152\n"
    "s_nop 0\n"
    "global_load_lds_dwordx4 v96, s[48:49]\n"
    "s_add_u32 m0, s61, 50176\n"
    "s_nop 0\n"
    "global_load_lds_dwordx4 v97, s[48:49]\n"
    "s_add_u32 m0, s61, 51200\n"
    "s_add_u32 s48, s48, 16384\n"
    "s_addc_u32 s49, s49, 0\n"
    "global_load_lds_dwordx4 v96, s[48:49]\n"
    "s_add_u32 m0, s61, 52224\n"
    "s_nop 0\n"
    "global_load_lds_dwordx4 v97, s[48:49]\n"
    "s_mul_i32 s58, s57, 196608\n"
    "s_add_u32 m0, s62, 90112\n"
    "s_add_u32 s56, s58, 0\n"
    "s_add_u32 s48, s66, s56\n"
    "s_addc_u32 s49, s67, 0\n"
    "global_load_lds_dwordx4 v98, s[48:49]\n"
    "s_add_u32 m0, s62, 91136\n"
    "s_add_u32 s56, s58, 24576\n"
    "s_add_u32 s48, s66, s56\n"
    "s_addc_u32 s49, s67, 0\n"
    "global_load_lds_dwordx4 v98, s[48:49]\n"
    "ds_read_b128 v[128:131], v99 offset:0\n"
    "ds_read_b128 v[132:135], v100 offset:0\n"
    "ds_read_b128 v[136:139], v101 offset:0\n"
    "ds_read_b128 v[140:143], v102 offset:0\n"
    "s_and_b32 s56, s28, 1\n"
    "s_lshl_b32 s56, s56, 10\n"
    "s_add_u32 m0, s56, 157696\n"
    "s_lshl_b32 s57, s32, 11\n"
    "s_add_u32 s56, s56, s57\n"
    "s_add_u32 s48, s16, s56\n"
    "s_addc_u32 s49, s17, 0\n"
    "global_load_lds_dwordx4 v105, s[48:49]\n"
    "s_lshl1_add_u32 s56, s32, s33\n"
    "s_lshl2_add_u32 s56, s56, s28\n"
    "s_lshl_b32 s57, s34, 6\n"
    "s_add_u32 s71, s56, s57\n"
    "s_lshl_b32 s56, s71, 11\n"
    "s_add_u32 s48, s8, s56\n"
    "s_addc_u32 s49, s9, 0\n"
    "global_load_dwordx4 v[112:115], v105, s[48:49]\n"
    "global_load_dwordx4 v[116:119], v105, s[48:49] offset:1024\n"
    "global_load_dwordx4 v[120:123], v105, s[20:21]\n"
    "v_bfe_u32 v124, v106, 2, 2\n"
    "v_lshrrev_b32 v0, 5, v106\n"
    "v_lshl_or_b32 v124, v0, 2, v124\n"
    "v_lshlrev_b32 v124, 5, v124\n"
    "v_and_b32 v0, 3, v106\n"
    "v_lshl_or_b32 v124, v0, 3, v124\n"
    "v_add_u32 v124, 157696, v124\n"
    "s_waitcnt vmcnt(17) lgkmcnt(0)\n"
    "s_barrier\n"
    "s_add_u32 s57, s35, 4\n"
    "s_and_b32 s57, s57, 7\n"
    "s_lshl_b32 s56, s57, 7\n"
    "s_add_u32 s48, s64, s56\n"
    "s_addc_u32 s49, s65, 0\n"
    "s_add_u32 m0, s61, 0\n"
    "s_nop 0\n"
    "global_load_lds_dwordx4 v96, s[48:49]\n"
    "s_add_u32 m0, s61, 1024\n"
    "s_nop 0\n"
    "global_load_lds_dwordx4 v97, s[48:49]\n"
    "s_add_u32 m0, s61, 2048\n"
    "s_add_u32 s48, s48, 16384\n"
    "s_addc_u32 s49, s49, 0\n"
    "global_load_lds_dwordx4 v96, s[48:49]\n"
    "s_add_u32 m0, s61, 3072\n"
    "s_nop 0\n"
    "global_load_lds_dwordx4 v97, s[48:49]\n"
    "s_mul_i32 s58, s57, 196608\n"
    "s_add_u32 m0, s62, 65536\n"
    "s_add_u32 s56, s58, 0\n"
    "s_add_u32 s48, s66, s56\n"
    "s_addc_u32 s49, s67, 0\n"
    "global_load_lds_dwordx4 v98, s[48:49]\n"
    "s_add_u32 m0, s62, 66560\n"
    "s_add_u32 s56, s58, 24576\n"
    "s_add_u32 s48, s66, s56\n"
    "s_addc_u32 s49, s67, 0\n"
    "global_load_lds_dwordx4 v98, s[48:49]\n"
    "ds_read_b128 v[144:147], v99 offset:16384\n"
    "ds_read_b128 v[148:151], v100 offset:16384\n"
    "ds_read_b128 v[152:155], v101 offset:16384\n"
    "ds_read_b128 v[156:159], v102 offset:16384\n"
    "s_waitcnt vmcnt(16) lgkmcnt(0)\n"
    "s_barrier\n"
    "s_add_u32 s57, s35, 5\n"
    "s_and_b32 s57, s57, 7\n"
    "s_lshl_b32 s56, s57, 7\n"
    "s_add_u32 s48, s64, s56\n"
    "s_addc_u32 s49, s65, 0\n"
    "s_add_u32 m0, s61, 16384\n"
    "s_nop 0\n"
    "global_load_lds_dwordx4 v96, s[48:49]\n"
    "s_add_u32 m0, s61, 17408\n"
    "s_nop 0\n"
    "global_load_lds_dwordx4 v97, s[48:49]\n"
    "s_add_u32 m0, s61, 18432\n"
    "s_add_u32 s48, s48, 16384\n"
    "s_addc_u32 s49, s49, 0\n"
    "global_load_lds_dwordx4 v96, s[48:49]\n"
    "s_add_u32 m0, s61, 19456\n"
    "s_nop 0\n"
    "global_load_lds_dwordx4 v97, s[48:49]\n"
    "s_mul_i32 s58, s57, 196608\n"
    "s_add_u32 m0, s62, 73728\n"
    "s_add_u32 s56, s58, 0\n"
    "s_add_u32 s48, s66, s56\n"
    "s_addc_u32 s49, s67, 0\n"
    "global_load_lds_dwordx4 v98, s[48:49]\n"
    "s_add_u32 m0, s62, 74752\n"
    "s_add_u32 s56, s58, 24576\n"
    "s_add_u32 s48, s66, s56\n"
    "s_addc_u32 s49, s67, 0\n"
    "global_load_lds_dwordx4 v98, s[48:49]\n"
    "ds_read_b128 v[160:163], v99 offset:32768\n"
    "ds_read_b128 v[164:167], v100 offset:32768\n"
    "ds_read_b128 v[168:171], v101 offset:32768\n"
    "ds_read_b128 v[172:175], v102 offset:32768\n"
    "s_waitcnt vmcnt(16) lgkmcnt(0)\n"
    "s_barrier\n"
    "s_add_u32 s57, s35, 6\n"
    "s_and_b32 s57, s57, 7\n"
    "s_lshl_b32 s56, s57, 7\n"
    "s_add_u32 s48, s64, s56\n"
    "s_addc_u32 s49, s65, 0\n"
    "s_add_u32 m0, s61, 32768\n"
    "s_nop 0\n"
    "global_load_lds_dwordx4 v96, s[48:49]\n"
    "s_add_u32 m0, s61, 33792\n"
    "s_nop 0\n"
    "global_load_lds_dwordx4 v97, s[48:49]\n"
    "s_add_u32 m0, s61, 34816\n"
    "s_add_u32 s48, s48, 16384\n"
    "s_addc_u32 s49, s49, 0\n"
    "global_load_lds_dwordx4 v96, s[48:49]\n"
    "s_add_u32 m0, s61, 35840\n"
    "s_nop 0\n"
    "global_load_lds_dwordx4 v97, s[48:49]\n"
    "s_mul_i32 s58, s57, 196608\n"
    "s_add_u32 m0, s62, 81920\n"
    "s_add_u32 s56, s58, 0\n"
    "s_add_u32 s48, s66, s56\n"
    "s_addc_u32 s49, s67, 0\n"
    "global_load_lds_dwordx4 v98, s[48:49]\n"
    "s_add_u32 m0, s62, 82944\n"
    "s_add_u32 s56, s58, 24576\n"
    "s_add_u32 s48, s66, s56\n"
    "s_addc_u32 s49, s67, 0\n"
    "global_load_lds_dwordx4 v98, s[48:49]\n"
    "ds_read_b128 v[176:179], v99 offset:49152\n"
    "ds_read_b128 v[180:183], v100 offset:49152\n"
    "ds_read_b128 v[184:187], v101 offset:49152\n"
    "ds_read_b128 v[188:191], v102 offset:49152\n"
    "s_waitcnt vmcnt(12) lgkmcnt(0)\n"
    "s_barrier\n"
    "s_add_u32 s57, s35, 7\n"
    "s_and_b32 s57, s57, 7\n"
    "s_lshl_b32 s56, s57, 7\n"
    "s_add_u32 s48, s64, s56\n"
    "s_addc_u32 s49, s65, 0\n"
    "s_add_u32 m0, s61, 49152\n"
    "s_nop 0\n"
    "global_load_lds_dwordx4 v96, s[48:49]\n"
    "s_add_u32 m0, s61, 50176\n"
    "s_nop 0\n"
    "global_load_lds_dwordx4 v97, s[48:49]\n"
    "s_add_u32 m0, s61, 51200\n"
    "s_add_u32 s48, s48, 16384\n"
    "s_addc_u32 s49, s49, 0\n"
    "global_load_lds_dwordx4 v96, s[48:49]\n"
    "s_add_u32 m0, s61, 52224\n"
    "s_nop 0\n"
    "global_load_lds_dwordx4 v97, s[48:49]\n"
    "s_mul_i32 s58, s57, 196608\n"
    "s_add_u32 m0, s62, 90112\n"
    "s_add_u32 s56, s58, 0\n"
    "s_add_u32 s48, s66, s56\n"
    "s_addc_u32 s49, s67, 0\n"
    "global_load_lds_dwordx4 v98, s[48:49]\n"
    "s_add_u32 m0, s62, 91136\n"
    "s_add_u32 s56, s58, 24576\n"
    "s_add_u32 s48, s66, s56\n"
    "s_addc_u32 s49, s67, 0\n"
    "global_load_lds_dwordx4 v98, s[48:49]\n"
    "s_add_u32 s57, s35, 0\n"
    "s_and_b32 s57, s57, 7\n"
    "s_mul_i32 s58, s57, 196608\n"
    "s_add_u32 m0, s62, 98304\n"
    "s_add_u32 s56, s58, 0\n"
    "s_add_u32 s48, s68, s56\n"
    "s_addc_u32 s49, s69, 0\n"
    "global_load_lds_dwordx4 v98, s[48:49]\n"
    "s_add_u32 m0, s62, 99328\n"
    "s_add_u32 s56, s58, 24576\n"
    "s_add_u32 s48, s68, s56\n"
    "s_addc_u32 s49, s69, 0\n"
    "global_load_lds_dwordx4 v98, s[48:49]\n"
    "s_add_u32 m0, s62, 106496\n"
    "s_add_u32 s56, s58, 1024\n"
    "s_add_u32 s48, s68, s56\n"
    "s_addc_u32 s49, s69, 0\n"
    "global_load_lds_dwordx4 v98, s[48:49]\n"
    "s_add_u32 m0, s62, 107520\n"
    "s_add_u32 s56, s58, 25600\n"
    "s_add_u32 s48, s68, s56\n"
    "s_addc_u32 s49, s69, 0\n"
    "global_load_lds_dwordx4 v98, s[48:49]\n"
    "ds_read_b128 v[192:195], v99 offset:0\n"
    "ds_read_b128 v[196:199], v100 offset:0\n"
    "ds_read_b128 v[200:203], v101 offset:0\n"
    "ds_read_b128 v[204:207], v102 offset:0\n"
    "v_dot2_f32_bf16 v0, v112, v120, 0\n"
    "v_dot2_f32_bf16 v1, v116, v120, 0\n"
    "v_dot2_f32_bf16 v0, v113, v121, v0\n"
    "v_dot2_f32_bf16 v1, v117, v121, v1\n"
    "v_dot2_f32_bf16 v0, v114, v122, v0\n"
    "v_dot2_f32_bf16 v1, v118, v122, v1\n"
    "v_dot2_f32_bf16 v0, v115, v123, v0\n"
    "v_dot2_f32_bf16 v1, v119, v123, v1\n"
    "s_nop 2\n"
    "v_permlane32_swap_b32 v0, v1\n"
    "v_add_f32 v0, v0, v1\n"
    "s_nop 1\n"
    "v_add_f32_dpp v0, v0, v0 row_ror:8 row_mask:0xf bank_mask:0xf\n"
    "s_nop 1\n"
    "v_add_f32_dpp v0, v0, v0 row_ror:4 row_mask:0xf bank_mask:0xf\n"
    "s_nop 1\n"
    "v_add_f32_dpp v0, v0, v0 row_ror:2 row_mask:0xf bank_mask:0xf\n"
    "s_nop 1\n"
    "v_add_f32_dpp v0, v0, v0 row_ror:1 row_mask:0xf bank_mask:0xf\n"
    "v_mov_b32 v1, v0\n"
    "s_nop 1\n"
    "v_permlane16_swap_b32 v0, v1\n"
    "v_add_f32 v0, v0, v1\n"
    "v_mul_f32 v0, 0xbfb8aa3b, v0\n"
    "v_exp_f32 v0, v0\n"
    "v_lshrrev_b32 v114, 3, v106\n"
    "v_add_f32 v0, 1.0, v0\n"
    "v_rcp_f32 v113, v0\n"
    "s_waitcnt vmcnt(16) lgkmcnt(0)\n"
    "s_barrier\n"
    "s_add_u32 s57, s35, 1\n"
    "s_and_b32 s57, s57, 7\n"
    "s_mul_i32 s58, s57, 196608\n"
    "s_add_u32 m0, s62, 114688\n"
    "s_add_u32 s56, s58, 0\n"
    "s_add_u32 s48, s68, s56\n"
    "s_addc_u32 s49, s69, 0\n"
    "global_load_lds_dwordx4 v98, s[48:49]\n"
    "s_add_u32 m0, s62, 115712\n"
    "s_add_u32 s56, s58, 24576\n"
    "s_add_u32 s48, s68, s56\n"
    "s_addc_u32 s49, s69, 0\n"
    "global_load_lds_dwordx4 v98, s[48:49]\n"
    "s_add_u32 m0, s62, 122880\n"
    "s_add_u32 s56, s58, 1024\n"
    "s_add_u32 s48, s68, s56\n"
    "s_addc_u32 s49, s69, 0\n"
    "global_load_lds_dwordx4 v98, s[48:49]\n"
    "s_add_u32 m0, s62, 123904\n"
    "s_add_u32 s56, s58, 25600\n"
    "s_add_u32 s48, s68, s56\n"
    "s_addc_u32 s49, s69, 0\n"
    "global_load_lds_dwordx4 v98, s[48:49]\n"
    "s_add_u32 s57, s35, 2\n"
    "s_and_b32 s57, s57, 7\n"
    "s_mul_i32 s58, s57, 196608\n"
    "s_add_u32 m0, s62, 131072\n"
    "s_add_u32 s56, s58, 0\n"
    "s_add_u32 s48, s68, s56\n"
    "s_addc_u32 s49, s69, 0\n"
    "global_load_lds_dwordx4 v98, s[48:49]\n"
    "s_add_u32 m0, s62, 132096\n"
    "s_add_u32 s56, s58, 24576\n"
    "s_add_u32 s48, s68, s56\n"
    "s_addc_u32 s49, s69, 0\n"
    "global_load_lds_dwordx4 v98, s[48:49]\n"
    "s_add_u32 m0, s62, 139264\n"
    "s_add_u32 s56, s58, 1024\n"
    "s_add_u32 s48, s68, s56\n"
    "s_addc_u32 s49, s69, 0\n"
    "global_load_lds_dwordx4 v98, s[48:49]\n"
    "s_add_u32 m0, s62, 140288\n"
    "s_add_u32 s56, s58, 25600\n"
    "s_add_u32 s48, s68, s56\n"
    "s_addc_u32 s49, s69, 0\n"
    "global_load_lds_dwordx4 v98, s[48:49]\n"
    "ds_read_b128 v[208:211], v99 offset:16384\n"
    "ds_read_b128 v[212:215], v100 offset:16384\n"
    "ds_read_b128 v[216:219], v101 offset:16384\n"
    "ds_read_b128 v[220:223], v102 offset:16384\n"
    "v_and_b32 v115, 31, v106\n"
    "v_lshrrev_b32 v116, 1, v115\n"
    "v_and_b32 v116, 3, v116\n"
    "v_lshrrev_b32 v121, 5, v106\n"
    "v_lshlrev_b32 v122, 1, v121\n"
    "v_xor_b32 v116, v116, v122\n"
    "v_lshlrev_b32 v116, 4, v116\n"
    "v_lshl_or_b32 v115, v115, 6, v116\n"
    "s_lshl_b32 s56, s28, 11\n"
    "s_add_u32 s56, s56, 65536\n"
    "v_add_u32 v115, s56, v115\n"
    "v_xor_b32 v116, 16, v115\n"
    "s_add_u32 s56, s56, 32768\n"
    "v_add_u32 v117, s56, v105\n"
    "v_bfe_u32 v118, v106, 2, 2\n"
    "v_lshl_or_b32 v118, v121, 2, v118\n"
    "v_bfe_u32 v119, v106, 4, 1\n"
    "v_bfe_u32 v120, v106, 1, 1\n"
    "v_lshl_or_b32 v119, v119, 1, v120\n"
    "v_lshrrev_b32 v120, 1, v118\n"
    "v_xor_b32 v119, v119, v120\n"
    "v_lshlrev_b32 v119, 4, v119\n"
    "v_lshl_or_b32 v118, v118, 6, v119\n"
    "v_and_b32 v119, 1, v106\n"
    "v_lshl_or_b32 v118, v119, 3, v118\n"
    "s_lshl_b32 s56, s28, 13\n"
    "s_add_u32 s56, s56, 65536\n"
    "v_add_u32 v118, s56, v118\n"
    "s_lshr_b32 s56, s28, 1\n"
    "s_lshl_b32 s56, s56, 13\n"
    "s_add_u32 s56, s56, 98304\n"
    "v_add_u32 v119, s56, v105\n"
    "s_and_b32 s56, s28, 1\n"
    "s_lshl_b32 s56, s56, 4\n"
    "v_and_b32 v120, 31, v106\n"
    "v_xor_b32 v120, s56, v120\n"
    "v_mad_u32_u24 v120, v121, 17, v120\n"
    "v_lshlrev_b32 v120, 4, v120\n"
    "s_waitcnt vmcnt(18) lgkmcnt(0)\n"
    "s_barrier\n"
    "s_add_u32 s57, s35, 3\n"
    "s_and_b32 s57, s57, 7\n"
    "s_mul_i32 s58, s57, 196608\n"
    "s_add_u32 m0, s62, 0\n"
    "s_add_u32 s56, s58, 0\n"
    "s_add_u32 s48, s68, s56\n"
    "s_addc_u32 s49, s69, 0\n"
    "global_load_lds_dwordx4 v98, s[48:49]\n"
    "s_add_u32 m0, s62, 1024\n"
    "s_add_u32 s56, s58, 24576\n"
    "s_add_u32 s48, s68, s56\n"
    "s_addc_u32 s49, s69, 0\n"
    "global_load_lds_dwordx4 v98, s[48:49]\n"
    "s_add_u32 m0, s62, 8192\n"
    "s_add_u32 s56, s58, 1024\n"
    "s_add_u32 s48, s68, s56\n"
    "s_addc_u32 s49, s69, 0\n"
    "global_load_lds_dwordx4 v98, s[48:49]\n"
    "s_add_u32 m0, s62, 9216\n"
    "s_add_u32 s56, s58, 25600\n"
    "s_add_u32 s48, s68, s56\n"
    "s_addc_u32 s49, s69, 0\n"
    "global_load_lds_dwordx4 v98, s[48:49]\n"
    "s_add_u32 s57, s35, 4\n"
    "s_and_b32 s57, s57, 7\n"
    "s_mul_i32 s58, s57, 196608\n"
    "s_add_u32 m0, s62, 16384\n"
    "s_add_u32 s56, s58, 0\n"
    "s_add_u32 s48, s68, s56\n"
    "s_addc_u32 s49, s69, 0\n"
    "global_load_lds_dwordx4 v98, s[48:49]\n"
    "s_add_u32 m0, s62, 17408\n"
    "s_add_u32 s56, s58, 24576\n"
    "s_add_u32 s48, s68, s56\n"
    "s_addc_u32 s49, s69, 0\n"
    "global_load_lds_dwordx4 v98, s[48:49]\n"
    "s_add_u32 m0, s62, 24576\n"
    "s_add_u32 s56, s58, 1024\n"
    "s_add_u32 s48, s68, s56\n"
    "s_addc_u32 s49, s69, 0\n"
    "global_load_lds_dwordx4 v98, s[48:49]\n"
    "s_add_u32 m0, s62, 25600\n"
    "s_add_u32 s56, s58, 25600\n"
    "s_add_u32 s48, s68, s56\n"
    "s_addc_u32 s49, s69, 0\n"
    "global_load_lds_dwordx4 v98, s[48:49]\n"
    "ds_read_b128 v[224:227], v99 offset:32768\n"
    "ds_read_b128 v[228:231], v100 offset:32768\n"
    "ds_read_b128 v[232:235], v101 offset:32768\n"
    "ds_read_b128 v[236:239], v102 offset:32768\n"
    "s_waitcnt vmcnt(20) lgkmcnt(0)\n"
    "s_barrier\n"
    "s_add_u32 s57, s35, 5\n"
    "s_and_b32 s57, s57, 7\n"
    "s_mul_i32 s58, s57, 196608\n"
    "s_add_u32 m0, s62, 65536\n"
    "s_add_u32 s56, s58, 0\n"
    "s_add_u32 s48, s68, s56\n"
    "s_addc_u32 s49, s69, 0\n"
    "global_load_lds_dwordx4 v98, s[48:49]\n"
    "s_add_u32 m0, s62, 66560\n"
    "s_add_u32 s56, s58, 24576\n"
    "s_add_u32 s48, s68, s56\n"
    "s_addc_u32 s49, s69, 0\n"
    "global_load_lds_dwordx4 v98, s[48:49]\n"
    "s_add_u32 m0, s62, 73728\n"
    "s_add_u32 s56, s58, 1024\n"
    "s_add_u32 s48, s68, s56\n"
    "s_addc_u32 s49, s69, 0\n"
    "global_load_lds_dwordx4 v98, s[48:49]\n"
    "s_add_u32 m0, s62, 74752\n"
    "s_add_u32 s56, s58, 25600\n"
    "s_add_u32 s48, s68, s56\n"
    "s_addc_u32 s49, s69, 0\n"
    "global_load_lds_dwordx4 v98, s[48:49]\n"
    "s_add_u32 s57, s35, 6\n"
    "s_and_b32 s57, s57, 7\n"
    "s_mul_i32 s58, s57, 196608\n"
    "s_add_u32 m0, s62, 32768\n"
    "s_add_u32 s56, s58, 0\n"
    "s_add_u32 s48, s68, s56\n"
    "s_addc_u32 s49, s69, 0\n"
    "global_load_lds_dwordx4 v98, s[48:49]\n"
    "s_add_u32 m0, s62, 33792\n"
    "s_add_u32 s56, s58, 24576\n"
    "s_add_u32 s48, s68, s56\n"
    "s_addc_u32 s49, s69, 0\n"
    "global_load_lds_dwordx4 v98, s[48:49]\n"
    "s_add_u32 m0, s62, 40960\n"
    "s_add_u32 s56, s58, 1024\n"
    "s_add_u32 s48, s68, s56\n"
    "s_addc_u32 s49, s69, 0\n"
    "global_load_lds_dwordx4 v98, s[48:49]\n"
    "s_add_u32 m0, s62, 41984\n"
    "s_add_u32 s56, s58, 25600\n"
    "s_add_u32 s48, s68, s56\n"
    "s_addc_u32 s49, s69, 0\n"
    "global_load_lds_dwordx4 v98, s[48:49]\n"
    "s_add_u32 s57, s35, 7\n"
    "s_and_b32 s57, s57, 7\n"
    "s_mul_i32 s58, s57, 196608\n"
    "s_add_u32 m0, s62, 147456\n"
    "s_add_u32 s56, s58, 0\n"
    "s_add_u32 s48, s68, s56\n"
    "s_addc_u32 s49, s69, 0\n"
    "global_load_lds_dwordx4 v98, s[48:49]\n"
    "s_add_u32 m0, s62, 148480\n"
    "s_add_u32 s56, s58, 24576\n"
    "s_add_u32 s48, s68, s56\n"
    "s_addc_u32 s49, s69, 0\n"
    "global_load_lds_dwordx4 v98, s[48:49]\n"
    "s_add_u32 m0, s62, 81920\n"
    "s_add_u32 s56, s58, 1024\n"
    "s_add_u32 s48, s68, s56\n"
    "s_addc_u32 s49, s69, 0\n"
    "global_load_lds_dwordx4 v98, s[48:49]\n"
    "s_add_u32 m0, s62, 82944\n"
    "s_add_u32 s56, s58, 25600\n"
    "s_add_u32 s48, s68, s56\n"
    "s_addc_u32 s49, s69, 0\n"
    "global_load_lds_dwordx4 v98, s[48:49]\n"
    "ds_read_b128 v[240:243], v99 offset:49152\n"
    "ds_read_b128 v[244:247], v100 offset:49152\n"
    "ds_read_b128 v[248:251], v101 offset:49152\n"
    "ds_read_b128 v[252:255], v102 offset:49152\n"
    "s_waitcnt vmcnt(12) lgkmcnt(0)\n"
    "s_barrier\n"
    "ds_read_b64_tr_b16 v[64:65], v125 offset:32768\n"
    "ds_read_b64_tr_b16 v[66:67], v125 offset:33280\n"
    "ds_read_b64_tr_b16 v[68:69], v125 offset:33024\n"
    "ds_read_b64_tr_b16 v[70:71], v125 offset:33536\n"
    "ds_read_b64_tr_b16 v[72:73], v125 offset:40960\n"
    "ds_read_b64_tr_b16 v[74:75], v125 offset:41472\n"
    "ds_read_b64_tr_b16 v[76:77], v125 offset:41216\n"
    "ds_read_b64_tr_b16 v[78:79], v125 offset:41728\n"
    "s_waitcnt lgkmcnt(6)\n"
    "v_mfma_f32_32x32x16_bf16 v[0:15], v[64:67], v[128:131], 0\n"
    "ds_read_b64_tr_b16 v[80:81], v125 offset:34816\n"
    "ds_read_b64_tr_b16 v[82:83], v125 offset:35328\n"
    "s_waitcnt lgkmcnt(6)\n"
    "v_mfma_f32_32x32x16_bf16 v[16:31], v[68:71], v[128:131], 0\n"
    "ds_read_b64_tr_b16 v[84:85], v125 offset:35072\n"
    "ds_read_b64_tr_b16 v[86:87], v125 offset:35584\n"
    "s_waitcnt lgkmcnt(6)\n"
    "v_mfma_f32_32x32x16_bf16 v[32:47], v[128:131], v[72:75], 0\n"
    "ds_read_b64_tr_b16 v[88:89], v125 offset:43008\n"
    "ds_read_b64_tr_b16 v[90:91], v125 offset:43520\n"
    "s_waitcnt lgkmcnt(6)\n"
    "v_mfma_f32_32x32x16_bf16 v[48:63], v[128:131], v[76:79], 0\n"
    "ds_read_b64_tr_b16 v[92:93], v125 offset:43264\n"
    "ds_read_b64_tr_b16 v[94:95], v125 offset:43776\n"
    "s_waitcnt lgkmcnt(6)\n"
    "v_mfma_f32_32x32x16_bf16 v[0:15], v[80:83], v[132:135], v[0:15]\n"
    "ds_read_b64_tr_b16 v[64:65], v125 offset:36864\n"
    "ds_read_b64_tr_b16 v[66:67], v125 offset:37376\n"
    "s_waitcnt lgkmcnt(6)\n"
    "v_mfma_f32_32x32x16_bf16 v[16:31], v[84:87], v[132:135], v[16:31]\n"
    "ds_read_b64_tr_b16 v[68:69], v125 offset:37120\n"
    "ds_read_b64_tr_b16 v[70:71], v125 offset:37632\n"
    "s_waitcnt lgkmcnt(6)\n"
    "v_mfma_f32_32x32x16_bf16 v[32:47], v[132:135], v[88:91], v[32:47]\n"
    "ds_read_b64_tr_b16 v[72:73], v125 offset:45056\n"
    "ds_read_b64_tr_b16 v[74:75], v125 offset:45568\n"
    "s_waitcnt lgkmcnt(6)\n"
    "v_mfma_f32_32x32x16_bf16 v[48:63], v[132:135], v[92:95], v[48:63]\n"
    "ds_read_b64_tr_b16 v[76:77], v125 offset:45312\n"
    "ds_read_b64_tr_b16 v[78:79], v125 offset:45824\n"
    "s_waitcnt lgkmcnt(6)\n"
    "v_mfma_f32_32x32x16_bf16 v[0:15], v[64:67], v[136:139], v[0:15]\n"
    "ds_read_b64_tr_b16 v[80:81], v125 offset:38912\n"
    "ds_read_b64_tr_b16 v[82:83], v125 offset:39424\n"
    "s_waitcnt lgkmcnt(6)\n"
    "v_mfma_f32_32x32x16_bf16 v[16:31], v[68:71], v[136:139], v[16:31]\n"
    "ds_read_b64_tr_b16 v[84:85], v125 offset:39168\n"
    "ds_read_b64_tr_b16 v[86:87], v125 offset:39680\n"
    "s_waitcnt lgkmcnt(6)\n"
    "v_mfma_f32_32x32x16_bf16 v[32:47], v[136:139], v[72:75], v[32:47]\n"
    "ds_read_b64_tr_b16 v[88:89], v125 offset:47104\n"
    "ds_read_b64_tr_b16 v[90:91], v125 offset:47616\n"
    "s_waitcnt lgkmcnt(6)\n"
    "v_mfma_f32_32x32x16_bf16 v[48:63], v[136:139], v[76:79], v[48:63]\n"
    "ds_read_b64_tr_b16 v[92:93], v125 offset:47360\n"
    "ds_read_b64_tr_b16 v[94:95], v125 offset:47872\n"
    "s_waitcnt lgkmcnt(6)\n"
    "v_mfma_f32_32x32x16_bf16 v[0:15], v[80:83], v[140:143], v[0:15]\n"
    "ds_read_b64_tr_b16 v[64:65], v125 offset:49152\n"
    "ds_read_b64_tr_b16 v[66:67], v125 offset:49664\n"
    "s_waitcnt lgkmcnt(6)\n"
    "v_mfma_f32_32x32x16_bf16 v[16:31], v[84:87], v[140:143], v[16:31]\n"
    "ds_read_b64_tr_b16 v[68:69], v125 offset:49408\n"
    "ds_read_b64_tr_b16 v[70:71], v125 offset:49920\n"
    "s_waitcnt lgkmcnt(6)\n"
    "v_mfma_f32_32x32x16_bf16 v[32:47], v[140:143], v[88:91], v[32:47]\n"
    "ds_read_b64_tr_b16 v[72:73], v125 offset:57344\n"
    "ds_read_b64_tr_b16 v[74:75], v125 offset:57856\n"
    "s_waitcnt lgkmcnt(6)\n"
    "v_mfma_f32_32x32x16_bf16 v[48:63], v[140:143], v[92:95], v[48:63]\n"
    "ds_read_b64_tr_b16 v[76:77], v125 offset:57600\n"
    "ds_read_b64_tr_b16 v[78:79], v125 offset:58112\n"
    "s_waitcnt lgkmcnt(6)\n"
    "v_mfma_f32_32x32x16_bf16 v[0:15], v[64:67], v[144:147], v[0:15]\n"
    "ds_read_b64_tr_b16 v[80:81], v125 offset:51200\n"
    "ds_read_b64_tr_b16 v[82:83], v125 offset:51712\n"
    "s_waitcnt lgkmcnt(6)\n"
    "v_mfma_f32_32x32x16_bf16 v[16:31], v[68:71], v[144:147], v[16:31]\n"
    "ds_read_b64_tr_b16 v[84:85], v125 offset:51456\n"
    "ds_read_b64_tr_b16 v[86:87], v125 offset:51968\n"
    "s_waitcnt lgkmcnt(6)\n"
    "v_mfma_f32_32x32x16_bf16 v[32:47], v[144:147], v[72:75], v[32:47]\n"
    "ds_read_b64_tr_b16 v[88:89], v125 offset:59392\n"
    "ds_read_b64_tr_b16 v[90:91], v125 offset:59904\n"
    "s_waitcnt lgkmcnt(6)\n"
    "v_mfma_f32_32x32x16_bf16 v[48:63], v[144:147], v[76:79], v[48:63]\n"
    "ds_read_b64_tr_b16 v[92:93], v125 offset:59648\n"
    "ds_read_b64_tr_b16 v[94:95], v125 offset:60160\n"
    "s_waitcnt lgkmcnt(6)\n"
    "v_mfma_f32_32x32x16_bf16 v[0:15], v[80:83], v[148:151], v[0:15]\n"
    "ds_read_b64_tr_b16 v[64:65], v125 offset:53248\n"
    "ds_read_b64_tr_b16 v[66:67], v125 offset:53760\n"
    "s_waitcnt lgkmcnt(6)\n"
    "v_mfma_f32_32x32x16_bf16 v[16:31], v[84:87], v[148:151], v[16:31]\n"
    "ds_read_b64_tr_b16 v[68:69], v125 offset:53504\n"
    "ds_read_b64_tr_b16 v[70:71], v125 offset:54016\n"
    "s_waitcnt lgkmcnt(6)\n"
    "v_mfma_f32_32x32x16_bf16 v[32:47], v[148:151], v[88:91], v[32:47]\n"
    "ds_read_b64_tr_b16 v[72:73], v125 offset:61440\n"
    "ds_read_b64_tr_b16 v[74:75], v125 offset:61952\n"
    "s_waitcnt lgkmcnt(6)\n"
    "v_mfma_f32_32x32x16_bf16 v[48:63], v[148:151], v[92:95], v[48:63]\n"
    "ds_read_b64_tr_b16 v[76:77], v125 offset:61696\n"
    "ds_read_b64_tr_b16 v[78:79], v125 offset:62208\n"
    "s_waitcnt lgkmcnt(6)\n"
    "v_mfma_f32_32x32x16_bf16 v[0:15], v[64:67], v[152:155], v[0:15]\n"
    "ds_read_b64_tr_b16 v[80:81], v125 offset:55296\n"
    "ds_read_b64_tr_b16 v[82:83], v125 offset:55808\n"
    "s_waitcnt lgkmcnt(6)\n"
    "v_mfma_f32_32x32x16_bf16 v[16:31], v[68:71], v[152:155], v[16:31]\n"
    "ds_read_b64_tr_b16 v[84:85], v125 offset:55552\n"
    "ds_read_b64_tr_b16 v[86:87], v125 offset:56064\n"
    "s_waitcnt lgkmcnt(6)\n"
    "v_mfma_f32_32x32x16_bf16 v[32:47], v[152:155], v[72:75], v[32:47]\n"
    "ds_read_b64_tr_b16 v[88:89], v125 offset:63488\n"
    "ds_read_b64_tr_b16 v[90:91], v125 offset:64000\n"
    "s_waitcnt lgkmcnt(6)\n"
    "v_mfma_f32_32x32x16_bf16 v[48:63], v[152:155], v[76:79], v[48:63]\n"
    "ds_read_b64_tr_b16 v[92:93], v125 offset:63744\n"
    "ds_read_b64_tr_b16 v[94:95], v125 offset:64256\n"
    "s_waitcnt lgkmcnt(6)\n"
    "v_mfma_f32_32x32x16_bf16 v[0:15], v[80:83], v[156:159], v[0:15]\n"
    "ds_read_b64_tr_b16 v[64:65], v126 offset:0\n"
    "ds_read_b64_tr_b16 v[66:67], v126 offset:512\n"
    "s_waitcnt lgkmcnt(6)\n"
    "v_mfma_f32_32x32x16_bf16 v[16:31], v[84:87], v[156:159], v[16:31]\n"
    "ds_read_b64_tr_b16 v[68:69], v126 offset:256\n"
    "ds_read_b64_tr_b16 v[70:71], v126 offset:768\n"
    "s_waitcnt lgkmcnt(6)\n"
    "v_mfma_f32_32x32x16_bf16 v[32:47], v[156:159], v[88:91], v[32:47]\n"
    "ds_read_b64_tr_b16 v[72:73], v126 offset:8192\n"
    "ds_read_b64_tr_b16 v[74:75], v126 offset:8704\n"
    "s_waitcnt lgkmcnt(6)\n"
    "v_mfma_f32_32x32x16_bf16 v[48:63], v[156:159], v[92:95], v[48:63]\n"
    "ds_read_b64_tr_b16 v[76:77], v126 offset:8448\n"
    "ds_read_b64_tr_b16 v[78:79], v126 offset:8960\n"
    "s_waitcnt lgkmcnt(6)\n"
    "v_mfma_f32_32x32x16_bf16 v[0:15], v[64:67], v[160:163], v[0:15]\n"
    "ds_read_b64_tr_b16 v[80:81], v126 offset:2048\n"
    "ds_read_b64_tr_b16 v[82:83], v126 offset:2560\n"
    "s_waitcnt lgkmcnt(6)\n"
    "v_mfma_f32_32x32x16_bf16 v[16:31], v[68:71], v[160:163], v[16:31]\n"
    "ds_read_b64_tr_b16 v[84:85], v126 offset:2304\n"
    "ds_read_b64_tr_b16 v[86:87], v126 offset:2816\n"
    "s_waitcnt lgkmcnt(6)\n"
    "v_mfma_f32_32x32x16_bf16 v[32:47], v[160:163], v[72:75], v[32:47]\n"
    "ds_read_b64_tr_b16 v[88:89], v126 offset:10240\n"
    "ds_read_b64_tr_b16 v[90:91], v126 offset:10752\n"
    "s_waitcnt lgkmcnt(6)\n"
    "v_mfma_f32_32x32x16_bf16 v[48:63], v[160:163], v[76:79], v[48:63]\n"
    "ds_read_b64_tr_b16 v[92:93], v126 offset:10496\n"
    "ds_read_b64_tr_b16 v[94:95], v126 offset:11008\n"
    "s_waitcnt lgkmcnt(6)\n"
    "v_mfma_f32_32x32x16_bf16 v[0:15], v[80:83], v[164:167], v[0:15]\n"
    "ds_read_b64_tr_b16 v[64:65], v126 offset:4096\n"
    "ds_read_b64_tr_b16 v[66:67], v126 offset:4608\n"
    "s_waitcnt lgkmcnt(6)\n"
    "v_mfma_f32_32x32x16_bf16 v[16:31], v[84:87], v[164:167], v[16:31]\n"
    "ds_read_b64_tr_b16 v[68:69], v126 offset:4352\n"
    "ds_read_b64_tr_b16 v[70:71], v126 offset:4864\n"
    "s_waitcnt lgkmcnt(6)\n"
    "v_mfma_f32_32x32x16_bf16 v[32:47], v[164:167], v[88:91], v[32:47]\n"
    "ds_read_b64_tr_b16 v[72:73], v126 offset:12288\n"
    "ds_read_b64_tr_b16 v[74:75], v126 offset:12800\n"
    "s_waitcnt lgkmcnt(6)\n"
    "v_mfma_f32_32x32x16_bf16 v[48:63], v[164:167], v[92:95], v[48:63]\n"
    "ds_read_b64_tr_b16 v[76:77], v126 offset:12544\n"
    "ds_read_b64_tr_b16 v[78:79], v126 offset:13056\n"
    "s_waitcnt lgkmcnt(6)\n"
    "v_mfma_f32_32x32x16_bf16 v[0:15], v[64:67], v[168:171], v[0:15]\n"
    "ds_read_b64_tr_b16 v[80:81], v126 offset:6144\n"
    "ds_read_b64_tr_b16 v[82:83], v126 offset:6656\n"
    "s_waitcnt lgkmcnt(6)\n"
    "v_mfma_f32_32x32x16_bf16 v[16:31], v[68:71], v[168:171], v[16:31]\n"
    "ds_read_b64_tr_b16 v[84:85], v126 offset:6400\n"
    "ds_read_b64_tr_b16 v[86:87], v126 offset:6912\n"
    "s_waitcnt lgkmcnt(6)\n"
    "v_mfma_f32_32x32x16_bf16 v[32:47], v[168:171], v[72:75], v[32:47]\n"
    "ds_read_b64_tr_b16 v[88:89], v126 offset:14336\n"
    "ds_read_b64_tr_b16 v[90:91], v126 offset:14848\n"
    "s_waitcnt lgkmcnt(6)\n"
    "v_mfma_f32_32x32x16_bf16 v[48:63], v[168:171], v[76:79], v[48:63]\n"
    "ds_read_b64_tr_b16 v[92:93], v126 offset:14592\n"
    "ds_read_b64_tr_b16 v[94:95], v126 offset:15104\n"
    "s_waitcnt lgkmcnt(6)\n"
    "v_mfma_f32_32x32x16_bf16 v[0:15], v[80:83], v[172:175], v[0:15]\n"
    "ds_read_b64_tr_b16 v[64:65], v103 offset:0\n"
    "ds_read_b64_tr_b16 v[66:67], v103 offset:512\n"
    "s_waitcnt lgkmcnt(6)\n"
    "v_mfma_f32_32x32x16_bf16 v[16:31], v[84:87], v[172:175], v[16:31]\n"
    "ds_read_b64_tr_b16 v[68:69], v103 offset:256\n"
    "ds_read_b64_tr_b16 v[70:71], v103 offset:768\n"
    "s_waitcnt lgkmcnt(6)\n"
    "v_mfma_f32_32x32x16_bf16 v[32:47], v[172:175], v[88:91], v[32:47]\n"
    "ds_read_b64_tr_b16 v[72:73], v103 offset:8192\n"
    "ds_read_b64_tr_b16 v[74:75], v103 offset:8704\n"
    "s_waitcnt lgkmcnt(6)\n"
    "v_mfma_f32_32x32x16_bf16 v[48:63], v[172:175], v[92:95], v[48:63]\n"
    "ds_read_b64_tr_b16 v[76:77], v103 offset:8448\n"
    "ds_read_b64_tr_b16 v[78:79], v103 offset:8960\n"
    "s_waitcnt lgkmcnt(6)\n"
    "v_mfma_f32_32x32x16_bf16 v[0:15], v[64:67], v[176:179], v[0:15]\n"
    "ds_read_b64_tr_b16 v[80:81], v103 offset:2048\n"
    "ds_read_b64_tr_b16 v[82:83], v103 offset:2560\n"
    "s_waitcnt lgkmcnt(6)\n"
    "v_mfma_f32_32x32x16_bf16 v[16:31], v[68:71], v[176:179], v[16:31]\n"
    "ds_read_b64_tr_b16 v[84:85], v103 offset:2304\n"
    "ds_read_b64_tr_b16 v[86:87], v103 offset:2816\n"
    "s_waitcnt lgkmcnt(6)\n"
    "v_mfma_f32_32x32x16_bf16 v[32:47], v[176:179], v[72:75], v[32:47]\n"
    "ds_read_b64_tr_b16 v[88:89], v103 offset:10240\n"
    "ds_read_b64_tr_b16 v[90:91], v103 offset:10752\n"
    "s_waitcnt lgkmcnt(6)\n"
    "v_mfma_f32_32x32x16_bf16 v[48:63], v[176:179], v[76:79], v[48:63]\n"
    "ds_read_b64_tr_b16 v[92:93], v103 offset:10496\n"
    "ds_read_b64_tr_b16 v[94:95], v103 offset:11008\n"
    "s_waitcnt lgkmcnt(6)\n"
    "v_mfma_f32_32x32x16_bf16 v[0:15], v[80:83], v[180:183], v[0:15]\n"
    "ds_read_b64_tr_b16 v[64:65], v103 offset:4096\n"
    "ds_read_b64_tr_b16 v[66:67], v103 offset:4608\n"
    "s_waitcnt lgkmcnt(6)\n"
    "v_mfma_f32_32x32x16_bf16 v[16:31], v[84:87], v[180:183], v[16:31]\n"
    "ds_read_b64_tr_b16 v[68:69], v103 offset:4352\n"
    "ds_read_b64_tr_b16 v[70:71], v103 offset:4864\n"
    "s_waitcnt lgkmcnt(6)\n"
    "v_mfma_f32_32x32x16_bf16 v[32:47], v[180:183], v[88:91], v[32:47]\n"
    "ds_read_b64_tr_b16 v[72:73], v103 offset:12288\n"
    "ds_read_b64_tr_b16 v[74:75], v103 offset:12800\n"
    "s_waitcnt lgkmcnt(6)\n"
    "v_mfma_f32_32x32x16_bf16 v[48:63], v[180:183], v[92:95], v[48:63]\n"
    "ds_read_b64_tr_b16 v[76:77], v103 offset:12544\n"
    "ds_read_b64_tr_b16 v[78:79], v103 offset:13056\n"
    "s_waitcnt lgkmcnt(6)\n"
    "v_mfma_f32_32x32x16_bf16 v[0:15], v[64:67], v[184:187], v[0:15]\n"
    "ds_read_b64_tr_b16 v[80:81], v103 offset:6144\n"
    "ds_read_b64_tr_b16 v[82:83], v103 offset:6656\n"
    "s_waitcnt lgkmcnt(6)\n"
    "v_mfma_f32_32x32x16_bf16 v[16:31], v[68:71], v[184:187], v[16:31]\n"
    "ds_read_b64_tr_b16 v[84:85], v103 offset:6400\n"
    "ds_read_b64_tr_b16 v[86:87], v103 offset:6912\n"
    "s_waitcnt lgkmcnt(6)\n"
    "v_mfma_f32_32x32x16_bf16 v[32:47], v[184:187], v[72:75], v[32:47]\n"
    "ds_read_b64_tr_b16 v[88:89], v103 offset:14336\n"
    "ds_read_b64_tr_b16 v[90:91], v103 offset:14848\n"
    "s_waitcnt lgkmcnt(6)\n"
    "v_mfma_f32_32x32x16_bf16 v[48:63], v[184:187], v[76:79], v[48:63]\n"
    "ds_read_b64_tr_b16 v[92:93], v103 offset:14592\n"
    "ds_read_b64_tr_b16 v[94:95], v103 offset:15104\n"
    "s_waitcnt lgkmcnt(6)\n"
    "v_mfma_f32_32x32x16_bf16 v[0:15], v[80:83], v[188:191], v[0:15]\n"
    "ds_read_b64_tr_b16 v[64:65], v103 offset:16384\n"
    "ds_read_b64_tr_b16 v[66:67], v103 offset:16896\n"
    "s_waitcnt lgkmcnt(6)\n"
    "v_mfma_f32_32x32x16_bf16 v[16:31], v[84:87], v[188:191], v[16:31]\n"
    "ds_read_b64_tr_b16 v[68:69], v103 offset:16640\n"
    "ds_read_b64_tr_b16 v[70:71], v103 offset:17152\n"
    "s_waitcnt lgkmcnt(6)\n"
    "v_mfma_f32_32x32x16_bf16 v[32:47], v[188:191], v[88:91], v[32:47]\n"
    "ds_read_b64_tr_b16 v[72:73], v103 offset:24576\n"
    "ds_read_b64_tr_b16 v[74:75], v103 offset:25088\n"
    "s_waitcnt lgkmcnt(6)\n"
    "v_mfma_f32_32x32x16_bf16 v[48:63], v[188:191], v[92:95], v[48:63]\n"
    "ds_read_b64_tr_b16 v[76:77], v103 offset:24832\n"
    "ds_read_b64_tr_b16 v[78:79], v103 offset:25344\n"
    "s_waitcnt lgkmcnt(6)\n"
    "v_mfma_f32_32x32x16_bf16 v[0:15], v[64:67], v[192:195], v[0:15]\n"
    "ds_read_b64_tr_b16 v[80:81], v103 offset:18432\n"
    "ds_read_b64_tr_b16 v[82:83], v103 offset:18944\n"
    "s_waitcnt lgkmcnt(6)\n"
    "v_mfma_f32_32x32x16_bf16 v[16:31], v[68:71], v[192:195], v[16:31]\n"
    "ds_read_b64_tr_b16 v[84:85], v103 offset:18688\n"
    "ds_read_b64_tr_b16 v[86:87], v103 offset:19200\n"
    "s_waitcnt lgkmcnt(6)\n"
    "v_mfma_f32_32x32x16_bf16 v[32:47], v[192:195], v[72:75], v[32:47]\n"
    "ds_read_b64_tr_b16 v[88:89], v103 offset:26624\n"
    "ds_read_b64_tr_b16 v[90:91], v103 offset:27136\n"
    "s_waitcnt lgkmcnt(6)\n"
    "v_mfma_f32_32x32x16_bf16 v[48:63], v[192:195], v[76:79], v[48:63]\n"
    "ds_read_b64_tr_b16 v[92:93], v103 offset:26880\n"
    "ds_read_b64_tr_b16 v[94:95], v103 offset:27392\n"
    "s_waitcnt lgkmcnt(6)\n"
    "v_mfma_f32_32x32x16_bf16 v[0:15], v[80:83], v[196:199], v[0:15]\n"
    "ds_read_b64_tr_b16 v[64:65], v103 offset:20480\n"
    "ds_read_b64_tr_b16 v[66:67], v103 offset:20992\n"
    "s_waitcnt lgkmcnt(6)\n"
    "v_mfma_f32_32x32x16_bf16 v[16:31], v[84:87], v[196:199], v[16:31]\n"
    "ds_read_b64_tr_b16 v[68:69], v103 offset:20736\n"
    "ds_read_b64_tr_b16 v[70:71], v103 offset:21248\n"
    "s_waitcnt lgkmcnt(6)\n"
    "v_mfma_f32_32x32x16_bf16 v[32:47], v[196:199], v[88:91], v[32:47]\n"
    "ds_read_b64_tr_b16 v[72:73], v103 offset:28672\n"
    "ds_read_b64_tr_b16 v[74:75], v103 offset:29184\n"
    "s_waitcnt lgkmcnt(6)\n"
    "v_mfma_f32_32x32x16_bf16 v[48:63], v[196:199], v[92:95], v[48:63]\n"
    "ds_read_b64_tr_b16 v[76:77], v103 offset:28928\n"
    "ds_read_b64_tr_b16 v[78:79], v103 offset:29440\n"
    "s_waitcnt lgkmcnt(6)\n"
    "v_mfma_f32_32x32x16_bf16 v[0:15], v[64:67], v[200:203], v[0:15]\n"
    "ds_read_b64_tr_b16 v[80:81], v103 offset:22528\n"
    "ds_read_b64_tr_b16 v[82:83], v103 offset:23040\n"
    "s_waitcnt lgkmcnt(6)\n"
    "v_mfma_f32_32x32x16_bf16 v[16:31], v[68:71], v[200:203], v[16:31]\n"
    "ds_read_b64_tr_b16 v[84:85], v103 offset:22784\n"
    "ds_read_b64_tr_b16 v[86:87], v103 offset:23296\n"
    "s_waitcnt lgkmcnt(6)\n"
    "v_mfma_f32_32x32x16_bf16 v[32:47], v[200:203], v[72:75], v[32:47]\n"
    "ds_read_b64_tr_b16 v[88:89], v103 offset:30720\n"
    "ds_read_b64_tr_b16 v[90:91], v103 offset:31232\n"
    "s_waitcnt lgkmcnt(6)\n"
    "v_mfma_f32_32x32x16_bf16 v[48:63], v[200:203], v[76:79], v[48:63]\n"
    "ds_read_b64_tr_b16 v[92:93], v103 offset:30976\n"
    "ds_read_b64_tr_b16 v[94:95], v103 offset:31488\n"
    "s_waitcnt vmcnt(0) lgkmcnt(0)\n"
    "s_barrier\n"
    "v_mfma_f32_32x32x16_bf16 v[0:15], v[80:83], v[204:207], v[0:15]\n"
    "ds_read_b64_tr_b16 v[64:65], v125 offset:0\n"
    "ds_read_b64_tr_b16 v[66:67], v125 offset:512\n"
    "v_mfma_f32_32x32x16_bf16 v[16:31], v[84:87], v[204:207], v[16:31]\n"
    "ds_read_b64_tr_b16 v[68:69], v125 offset:256\n"
    "ds_read_b64_tr_b16 v[70:71], v125 offset:768\n"
    "v_mfma_f32_32x32x16_bf16 v[32:47], v[204:207], v[88:91], v[32:47]\n"
    "ds_read_b64_tr_b16 v[72:73], v125 offset:8192\n"
    "ds_read_b64_tr_b16 v[74:75], v125 offset:8704\n"
    "v_mfma_f32_32x32x16_bf16 v[48:63], v[204:207], v[92:95], v[48:63]\n"
    "ds_read_b64_tr_b16 v[76:77], v125 offset:8448\n"
    "ds_read_b64_tr_b16 v[78:79], v125 offset:8960\n"
    "s_waitcnt lgkmcnt(6)\n"
    "v_mfma_f32_32x32x16_bf16 v[0:15], v[64:67], v[208:211], v[0:15]\n"
    "ds_read_b64_tr_b16 v[80:81], v125 offset:2048\n"
    "ds_read_b64_tr_b16 v[82:83], v125 offset:2560\n"
    "s_waitcnt lgkmcnt(6)\n"
    "v_mfma_f32_32x32x16_bf16 v[16:31], v[68:71], v[208:211], v[16:31]\n"
    "ds_read_b64_tr_b16 v[84:85], v125 offset:2304\n"
    "ds_read_b64_tr_b16 v[86:87], v125 offset:2816\n"
    "s_waitcnt lgkmcnt(6)\n"
    "v_mfma_f32_32x32x16_bf16 v[32:47], v[208:211], v[72:75], v[32:47]\n"
    "ds_read_b64_tr_b16 v[88:89], v125 offset:10240\n"
    "ds_read_b64_tr_b16 v[90:91], v125 offset:10752\n"
    "s_waitcnt lgkmcnt(6)\n"
    "v_mfma_f32_32x32x16_bf16 v[48:63], v[208:211], v[76:79], v[48:63]\n"
    "ds_read_b64_tr_b16 v[92:93], v125 offset:10496\n"
    "ds_read_b64_tr_b16 v[94:95], v125 offset:11008\n"
    "s_waitcnt lgkmcnt(6)\n"
    "v_mfma_f32_32x32x16_bf16 v[0:15], v[80:83], v[212:215], v[0:15]\n"
    "ds_read_b64_tr_b16 v[64:65], v125 offset:4096\n"
    "ds_read_b64_tr_b16 v[66:67], v125 offset:4608\n"
    "s_waitcnt lgkmcnt(6)\n"
    "v_mfma_f32_32x32x16_bf16 v[16:31], v[84:87], v[212:215], v[16:31]\n"
    "ds_read_b64_tr_b16 v[68:69], v125 offset:4352\n"
    "ds_read_b64_tr_b16 v[70:71], v125 offset:4864\n"
    "s_waitcnt lgkmcnt(6)\n"
    "v_mfma_f32_32x32x16_bf16 v[32:47], v[212:215], v[88:91], v[32:47]\n"
    "ds_read_b64_tr_b16 v[72:73], v125 offset:12288\n"
    "ds_read_b64_tr_b16 v[74:75], v125 offset:12800\n"
    "s_waitcnt lgkmcnt(6)\n"
    "v_mfma_f32_32x32x16_bf16 v[48:63], v[212:215], v[92:95], v[48:63]\n"
    "ds_read_b64_tr_b16 v[76:77], v125 offset:12544\n"
    "ds_read_b64_tr_b16 v[78:79], v125 offset:13056\n"
    "s_waitcnt lgkmcnt(6)\n"
    "v_mfma_f32_32x32x16_bf16 v[0:15], v[64:67], v[216:219], v[0:15]\n"
    "ds_read_b64_tr_b16 v[80:81], v125 offset:6144\n"
    "ds_read_b64_tr_b16 v[82:83], v125 offset:6656\n"
    "s_waitcnt lgkmcnt(6)\n"
    "v_mfma_f32_32x32x16_bf16 v[16:31], v[68:71], v[216:219], v[16:31]\n"
    "ds_read_b64_tr_b16 v[84:85], v125 offset:6400\n"
    "ds_read_b64_tr_b16 v[86:87], v125 offset:6912\n"
    "s_waitcnt lgkmcnt(6)\n"
    "v_mfma_f32_32x32x16_bf16 v[32:47], v[216:219], v[72:75], v[32:47]\n"
    "ds_read_b64_tr_b16 v[88:89], v125 offset:14336\n"
    "ds_read_b64_tr_b16 v[90:91], v125 offset:14848\n"
    "s_waitcnt lgkmcnt(6)\n"
    "v_mfma_f32_32x32x16_bf16 v[48:63], v[216:219], v[76:79], v[48:63]\n"
    "ds_read_b64_tr_b16 v[92:93], v125 offset:14592\n"
    "ds_read_b64_tr_b16 v[94:95], v125 offset:15104\n"
    "s_waitcnt lgkmcnt(6)\n"
    "v_mfma_f32_32x32x16_bf16 v[0:15], v[80:83], v[220:223], v[0:15]\n"
    "ds_read_b64_tr_b16 v[64:65], v103 offset:32768\n"
    "ds_read_b64_tr_b16 v[66:67], v103 offset:33280\n"
    "s_waitcnt lgkmcnt(6)\n"
    "v_mfma_f32_32x32x16_bf16 v[16:31], v[84:87], v[220:223], v[16:31]\n"
    "ds_read_b64_tr_b16 v[68:69], v103 offset:33024\n"
    "ds_read_b64_tr_b16 v[70:71], v103 offset:33536\n"
    "s_waitcnt lgkmcnt(6)\n"
    "v_mfma_f32_32x32x16_bf16 v[32:47], v[220:223], v[88:91], v[32:47]\n"
    "ds_read_b64_tr_b16 v[72:73], v103 offset:40960\n"
    "ds_read_b64_tr_b16 v[74:75], v103 offset:41472\n"
    "s_waitcnt lgkmcnt(6)\n"
    "v_mfma_f32_32x32x16_bf16 v[48:63], v[220:223], v[92:95], v[48:63]\n"
    "ds_read_b64_tr_b16 v[76:77], v103 offset:41216\n"
    "ds_read_b64_tr_b16 v[78:79], v103 offset:41728\n"
    "s_waitcnt lgkmcnt(6)\n"
    "v_mfma_f32_32x32x16_bf16 v[0:15], v[64:67], v[224:227], v[0:15]\n"
    "ds_read_b64_tr_b16 v[80:81], v103 offset:34816\n"
    "ds_read_b64_tr_b16 v[82:83], v103 offset:35328\n"
    "s_waitcnt lgkmcnt(6)\n"
    "v_mfma_f32_32x32x16_bf16 v[16:31], v[68:71], v[224:227], v[16:31]\n"
    "ds_read_b64_tr_b16 v[84:85], v103 offset:35072\n"
    "ds_read_b64_tr_b16 v[86:87], v103 offset:35584\n"
    "s_waitcnt lgkmcnt(6)\n"
    "v_mfma_f32_32x32x16_bf16 v[32:47], v[224:227], v[72:75], v[32:47]\n"
    "ds_read_b64_tr_b16 v[88:89], v103 offset:43008\n"
    "ds_read_b64_tr_b16 v[90:91], v103 offset:43520\n"
    "s_waitcnt lgkmcnt(6)\n"
    "v_mfma_f32_32x32x16_bf16 v[48:63], v[224:227], v[76:79], v[48:63]\n"
    "ds_read_b64_tr_b16 v[92:93], v103 offset:43264\n"
    "ds_read_b64_tr_b16 v[94:95], v103 offset:43776\n"
    "s_waitcnt lgkmcnt(6)\n"
    "v_mfma_f32_32x32x16_bf16 v[0:15], v[80:83], v[228:231], v[0:15]\n"
    "ds_read_b64_tr_b16 v[64:65], v103 offset:36864\n"
    "ds_read_b64_tr_b16 v[66:67], v103 offset:37376\n"
    "s_waitcnt lgkmcnt(6)\n"
    "v_mfma_f32_32x32x16_bf16 v[16:31], v[84:87], v[228:231], v[16:31]\n"
    "ds_read_b64_tr_b16 v[68:69], v103 offset:37120\n"
    "ds_read_b64_tr_b16 v[70:71], v103 offset:37632\n"
    "s_waitcnt lgkmcnt(6)\n"
    "v_mfma_f32_32x32x16_bf16 v[32:47], v[228:231], v[88:91], v[32:47]\n"
    "ds_read_b64_tr_b16 v[72:73], v103 offset:45056\n"
    "ds_read_b64_tr_b16 v[74:75], v103 offset:45568\n"
    "s_waitcnt lgkmcnt(6)\n"
    "v_mfma_f32_32x32x16_bf16 v[48:63], v[228:231], v[92:95], v[48:63]\n"
    "ds_read_b64_tr_b16 v[76:77], v103 offset:45312\n"
    "ds_read_b64_tr_b16 v[78:79], v103 offset:45824\n"
    "s_waitcnt lgkmcnt(6)\n"
    "v_mfma_f32_32x32x16_bf16 v[0:15], v[64:67], v[232:235], v[0:15]\n"
    "ds_read_b64_tr_b16 v[80:81], v103 offset:38912\n"
    "ds_read_b64_tr_b16 v[82:83], v103 offset:39424\n"
    "s_waitcnt lgkmcnt(6)\n"
    "v_mfma_f32_32x32x16_bf16 v[16:31], v[68:71], v[232:235], v[16:31]\n"
    "ds_read_b64_tr_b16 v[84:85], v103 offset:39168\n"
    "ds_read_b64_tr_b16 v[86:87], v103 offset:39680\n"
    "s_waitcnt lgkmcnt(6)\n"
    "v_mfma_f32_32x32x16_bf16 v[32:47], v[232:235], v[72:75], v[32:47]\n"
    "ds_read_b64_tr_b16 v[88:89], v103 offset:47104\n"
    "ds_read_b64_tr_b16 v[90:91], v103 offset:47616\n"
    "s_waitcnt lgkmcnt(6)\n"
    "v_mfma_f32_32x32x16_bf16 v[48:63], v[232:235], v[76:79], v[48:63]\n"
    "ds_read_b64_tr_b16 v[92:93], v103 offset:47360\n"
    "ds_read_b64_tr_b16 v[94:95], v103 offset:47872\n"
    "s_waitcnt lgkmcnt(6)\n"
    "v_mfma_f32_32x32x16_bf16 v[0:15], v[80:83], v[236:239], v[0:15]\n"
    "ds_read_b64_tr_b16 v[64:65], v126 offset:16384\n"
    "ds_read_b64_tr_b16 v[66:67], v126 offset:16896\n"
    "s_waitcnt lgkmcnt(6)\n"
    "v_mfma_f32_32x32x16_bf16 v[16:31], v[84:87], v[236:239], v[16:31]\n"
    "ds_read_b64_tr_b16 v[68:69], v126 offset:16640\n"
    "ds_read_b64_tr_b16 v[70:71], v126 offset:17152\n"
    "s_waitcnt lgkmcnt(6)\n"
    "v_mfma_f32_32x32x16_bf16 v[32:47], v[236:239], v[88:91], v[32:47]\n"
    "ds_read_b64_tr_b16 v[72:73], v125 offset:16384\n"
    "ds_read_b64_tr_b16 v[74:75], v125 offset:16896\n"
    "s_waitcnt lgkmcnt(6)\n"
    "v_mfma_f32_32x32x16_bf16 v[48:63], v[236:239], v[92:95], v[48:63]\n"
    "ds_read_b64_tr_b16 v[76:77], v125 offset:16640\n"
    "ds_read_b64_tr_b16 v[78:79], v125 offset:17152\n"
    "s_waitcnt lgkmcnt(6)\n"
    "v_mfma_f32_32x32x16_bf16 v[0:15], v[64:67], v[240:243], v[0:15]\n"
    "ds_read_b64_tr_b16 v[80:81], v126 offset:18432\n"
    "ds_read_b64_tr_b16 v[82:83], v126 offset:18944\n"
    "s_waitcnt lgkmcnt(6)\n"
    "v_mfma_f32_32x32x16_bf16 v[16:31], v[68:71], v[240:243], v[16:31]\n"
    "ds_read_b64_tr_b16 v[84:85], v126 offset:18688\n"
    "ds_read_b64_tr_b16 v[86:87], v126 offset:19200\n"
    "s_waitcnt lgkmcnt(6)\n"
    "v_mfma_f32_32x32x16_bf16 v[32:47], v[240:243], v[72:75], v[32:47]\n"
    "ds_read_b64_tr_b16 v[88:89], v125 offset:18432\n"
    "ds_read_b64_tr_b16 v[90:91], v125 offset:18944\n"
    "s_waitcnt lgkmcnt(6)\n"
    "v_mfma_f32_32x32x16_bf16 v[48:63], v[240:243], v[76:79], v[48:63]\n"
    "ds_read_b64_tr_b16 v[92:93], v125 offset:18688\n"
    "ds_read_b64_tr_b16 v[94:95], v125 offset:19200\n"
    "s_waitcnt lgkmcnt(6)\n"
    "v_mfma_f32_32x32x16_bf16 v[0:15], v[80:83], v[244:247], v[0:15]\n"
    "ds_read_b64_tr_b16 v[64:65], v126 offset:20480\n"
    "ds_read_b64_tr_b16 v[66:67], v126 offset:20992\n"
    "s_waitcnt lgkmcnt(6)\n"
    "v_mfma_f32_32x32x16_bf16 v[16:31], v[84:87], v[244:247], v[16:31]\n"
    "ds_read_b64_tr_b16 v[68:69], v126 offset:20736\n"
    "ds_read_b64_tr_b16 v[70:71], v126 offset:21248\n"
    "s_waitcnt lgkmcnt(6)\n"
    "v_mfma_f32_32x32x16_bf16 v[32:47], v[244:247], v[88:91], v[32:47]\n"
    "ds_read_b64_tr_b16 v[72:73], v125 offset:20480\n"
    "ds_read_b64_tr_b16 v[74:75], v125 offset:20992\n"
    "s_waitcnt lgkmcnt(6)\n"
    "v_mfma_f32_32x32x16_bf16 v[48:63], v[244:247], v[92:95], v[48:63]\n"
    "ds_read_b64_tr_b16 v[76:77], v125 offset:20736\n"
    "ds_read_b64_tr_b16 v[78:79], v125 offset:21248\n"
    "s_waitcnt lgkmcnt(6)\n"
    "v_mfma_f32_32x32x16_bf16 v[0:15], v[64:67], v[248:251], v[0:15]\n"
    "ds_read_b64_tr_b16 v[80:81], v126 offset:22528\n"
    "ds_read_b64_tr_b16 v[82:83], v126 offset:23040\n"
    "s_waitcnt lgkmcnt(6)\n"
    "v_mfma_f32_32x32x16_bf16 v[16:31], v[68:71], v[248:251], v[16:31]\n"
    "ds_read_b64_tr_b16 v[84:85], v126 offset:22784\n"
    "ds_read_b64_tr_b16 v[86:87], v126 offset:23296\n"
    "s_waitcnt lgkmcnt(6)\n"
    "v_mfma_f32_32x32x16_bf16 v[32:47], v[248:251], v[72:75], v[32:47]\n"
    "ds_read_b64_tr_b16 v[88:89], v125 offset:22528\n"
    "ds_read_b64_tr_b16 v[90:91], v125 offset:23040\n"
    "s_waitcnt lgkmcnt(6)\n"
    "v_mfma_f32_32x32x16_bf16 v[48:63], v[248:251], v[76:79], v[48:63]\n"
    "ds_read_b64_tr_b16 v[92:93], v125 offset:22784\n"
    "ds_read_b64_tr_b16 v[94:95], v125 offset:23296\n"
    "s_waitcnt lgkmcnt(6)\n"
    "v_mfma_f32_32x32x16_bf16 v[0:15], v[80:83], v[252:255], v[0:15]\n"
    "s_waitcnt lgkmcnt(4)\n"
    "v_mfma_f32_32x32x16_bf16 v[16:31], v[84:87], v[252:255], v[16:31]\n"
    "s_waitcnt lgkmcnt(2)\n"
    "v_mfma_f32_32x32x16_bf16 v[32:47], v[252:255], v[88:91], v[32:47]\n"
    "s_waitcnt lgkmcnt(0)\n"
    "v_mfma_f32_32x32x16_bf16 v[48:63], v[252:255], v[92:95], v[48:63]\n"
    "26: s_cbranch_execz 26f\n"
    "s_setprio 3\n"
    "v_readfirstlane_b32 s39, v104\n"
    "s_mov_b64 exec, 1\n"
    "ds_add_u32 v108, v109 offset:16\n"
    "s_mov_b64 exec, -1\n"
    "s_add_u32 s82, s39, 1\n"
    "s_xor_b32 s83, s82, 1540483477\n"
    "s_mov_b32 s54, 0x3fb8aa3b\n"
    "s_mov_b32 s55, 0x3fb8aa3b\n"
    "s_mov_b32 s58, 0x5d800000\n"
    "s_mov_b64 s[52:53], 0\n"
    "ds_read_b64_tr_b16 v[64:65], v124 offset:0\n"
    "ds_read_b64_tr_b16 v[66:67], v124 offset:256\n"
    "ds_read_b64_tr_b16 v[68:69], v124 offset:512\n"
    "ds_read_b64_tr_b16 v[70:71], v124 offset:768\n"
    "ds_read_b64_tr_b16 v[72:73], v124 offset:1024\n"
    "ds_read_b64_tr_b16 v[74:75], v124 offset:1280\n"
    "ds_read_b64_tr_b16 v[76:77], v124 offset:1536\n"
    "ds_read_b64_tr_b16 v[78:79], v124 offset:1792\n"
    "v_pk_mul_f32 v[0:1], v[0:1], s[54:55]\n"
    "v_pk_mul_f32 v[2:3], v[2:3], s[54:55]\n"
    "v_pk_mul_f32 v[4:5], v[4:5], s[54:55]\n"
    "v_pk_mul_f32 v[6:7], v[6:7], s[54:55]\n"
    "v_cvt_pk_bf16_f32 v80, v0, v1\n"
    "v_cvt_pk_bf16_f32 v81, v2, v3\n"
    "v_cvt_pk_bf16_f32 v82, v4, v5\n"
    "v_cvt_pk_bf16_f32 v83, v6, v7\n"
    "v_pk_mul_f32 v[8:9], v[8:9], s[54:55]\n"
    "v_pk_mul_f32 v[10:11], v[10:11], s[54:55]\n"
    "27: s_cbranch_execz 27f\n"
    "v_pk_mul_f32 v[12:13], v[12:13], s[54:55]\n"
    "v_pk_mul_f32 v[14:15], v[14:15], s[54:55]\n"
    "v_cvt_pk_bf16_f32 v84, v8, v9\n"
    "v_cvt_pk_bf16_f32 v85, v10, v11\n"
    "v_cvt_pk_bf16_f32 v86, v12, v13\n"
    "v_cvt_pk_bf16_f32 v87, v14, v15\n"
    "s_waitcnt lgkmcnt(0)\n"
    "v_mfma_f32_32x32x16_bf16 v[128:143], v[64:67], v[80:83], 0\n"
    "v_pk_mul_f32 v[16:17], v[16:17], s[54:55]\n"
    "v_pk_mul_f32 v[18:19], v[18:19], s[54:55]\n"
    "v_pk_mul_f32 v[20:21], v[20:21], s[54:55]\n"
    "v_pk_mul_f32 v[22:23], v[22:23], s[54:55]\n"
    "v_cvt_pk_bf16_f32 v88, v16, v17\n"
    "v_cvt_pk_bf16_f32 v89, v18, v19\n"
    "v_cvt_pk_bf16_f32 v90, v20, v21\n"
    "v_cvt_pk_bf16_f32 v91, v22, v23\n"
    "v_mfma_f32_32x32x16_bf16 v[144:159], v[68:71], v[84:87], 0\n"
    "v_pk_mul_f32 v[24:25], v[24:25], s[54:55]\n"
    "v_pk_mul_f32 v[26:27], v[26:27], s[54:55]\n"
    "v_pk_mul_f32 v[28:29], v[28:29], s[54:55]\n"
    "v_pk_mul_f32 v[30:31], v[30:31], s[54:55]\n"
    "v_cvt_pk_bf16_f32 v92, v24, v25\n"
    "v_cvt_pk_bf16_f32 v93, v26, v27\n"
    "v_cvt_pk_bf16_f32 v94, v28, v29\n"
    "v_cvt_pk_bf16_f32 v95, v30, v31\n"
    "v_mfma_f32_32x32x16_bf16 v[160:175], v[72:75], v[88:91], 0\n"
    "v_cvt_pk_bf16_f32 v224, v32, v33\n"
    "28: s_cbranch_execz 28f\n"
    "v_cvt_pk_bf16_f32 v225, v34, v35\n"
    "v_cvt_pk_bf16_f32 v226, v36, v37\n"
    "v_cvt_pk_bf16_f32 v227, v38, v39\n"
    "v_cvt_pk_bf16_f32 v228, v40, v41\n"
    "v_cvt_pk_bf16_f32 v229, v42, v43\n"
    "v_cvt_pk_bf16_f32 v230, v44, v45\n"
    "v_cvt_pk_bf16_f32 v231, v46, v47\n"
    "v_mfma_f32_32x32x16_bf16 v[176:191], v[76:79], v[92:95], 0\n"
    "v_cvt_pk_bf16_f32 v232, v48, v49\n"
    "v_cvt_pk_bf16_f32 v233, v50, v51\n"
    "v_cvt_pk_bf16_f32 v234, v52, v53\n"
    "v_cvt_pk_bf16_f32 v235, v54, v55\n"
    "v_cvt_pk_bf16_f32 v236, v56, v57\n"
    "v_cvt_pk_bf16_f32 v237, v58, v59\n"
    "v_cvt_pk_bf16_f32 v238, v60, v61\n"
    "v_cvt_pk_bf16_f32 v239, v62, v63\n"
    "v_mov_b32 v32, 0x3f803f80\n"
    "v_mov_b32 v33, v32\n"
    "v_mov_b64 v[34:35], v[32:33]\n"
    "v_mov_b64 v[36:37], v[32:33]\n"
    "v_mov_b64 v[38:39], v[32:33]\n"
    "v_mov_b64 v[40:41], v[32:33]\n"
    "v_mov_b64 v[42:43], v[32:33]\n"
    "v_mov_b64 v[44:45], v[32:33]\n"
    "v_mov_b64 v[46:47], v[32:33]\n"
    "v_mov_b64 v[48:49], v[32:33]\n"
    "v_mov_b64 v[50:51], v[32:33]\n"
    "29: s_cbranch_execz 29f\n"
    "v_mov_b64 v[52:53], v[32:33]\n"
    "v_mov_b64 v[54:55], v[32:33]\n"
    "v_mov_b64 v[56:57], v[32:33]\n"
    "v_mov_b64 v[58:59], v[32:33]\n"
    "v_mov_b64 v[60:61], v[32:33]\n"
    "v_mov_b64 v[62:63], v[32:33]\n"
    "s_bitcmp1_b32 s28, 0\n"
    "s_cselect_b32 s50, 0xffff0000, s46\n"
    "s_cselect_b32 s66, 1, 0x10000\n"
    "s_or_b32 s66, s66, s50\n"
    "s_mov_b32 s51, s50\n"
    "s_mov_b32 s67, s66\n"
    "s_lshl2_add_u32 s56, s32, s28\n"
    "s_lshl4_add_u32 s56, s56, s34\n"
    "s_mul_i32 s56, s56, 2176\n"
    "s_add_u32 s56, s56, 14680064\n"
    "s_add_u32 s64, s24, s56\n"
    "s_addc_u32 s65, s25, 0\n"
    "v_exp_f32 v136, -v128\n"
    "v_exp_f32 v152, -v144\n"
    "v_exp_f32 v168, -v160\n"
    "v_exp_f32 v184, -v176\n"
    "v_exp_f32 v137, -v129\n"
    "v_exp_f32 v153, -v145\n"
    "v_exp_f32 v169, -v161\n"
    "v_exp_f32 v185, -v177\n"
    "v_exp_f32 v138, -v130\n"
    "v_exp_f32 v154, -v146\n"
    "v_exp_f32 v170, -v162\n"
    "v_exp_f32 v186, -v178\n"
    "v_exp_f32 v139, -v131\n"
    "v_exp_f32 v155, -v147\n"
    "v_exp_f32 v171, -v163\n"
    "v_exp_f32 v187, -v179\n"
    "v_exp_f32 v140, -v132\n"
    "v_exp_f32 v156, -v148\n"
    "v_exp_f32 v172, -v164\n"
    "v_exp_f32 v188, -v180\n"
    "v_exp_f32 v141, -v133\n"
    "v_exp_f32 v157, -v149\n"
    "v_exp_f32 v173, -v165\n"
    "v_exp_f32 v189, -v181\n"
    "26: s_cbranch_execz 26f\n"
    "v_exp_f32 v142, -v134\n"
    "v_exp_f32 v158, -v150\n"
    "v_exp_f32 v174, -v166\n"
    "v_exp_f32 v190, -v182\n"
    "v_exp_f32 v143, -v135\n"
    "v_exp_f32 v159, -v151\n"
    "v_exp_f32 v175, -v167\n"
    "v_exp_f32 v191, -v183\n"
    "v_exp_f32 v128, v128\n"
    "v_exp_f32 v144, v144\n"
    "v_exp_f32 v160, v160\n"
    "v_exp_f32 v176, v176\n"
    "v_exp_f32 v129, v129\n"
    "v_exp_f32 v145, v145\n"
    "v_exp_f32 v161, v161\n"
    "v_exp_f32 v177, v177\n"
    "v_exp_f32 v130, v130\n"
    "v_exp_f32 v146, v146\n"
    "v_exp_f32 v162, v162\n"
    "v_exp_f32 v178, v178\n"
    "v_exp_f32 v131, v131\n"
    "v_exp_f32 v147, v147\n"
    "v_exp_f32 v163, v163\n"
    "v_exp_f32 v179, v179\n"
    "v_exp_f32 v132, v132\n"
    "v_exp_f32 v148, v148\n"
    "v_exp_f32 v164, v164\n"
    "v_exp_f32 v180, v180\n"
    "v_exp_f32 v133, v133\n"
    "v_exp_f32 v149, v149\n"
    "v_exp_f32 v165, v165\n"
    "v_exp_f32 v181, v181\n"
    "v_exp_f32 v134, v134\n"
    "v_exp_f32 v150, v150\n"
    "v_exp_f32 v166, v166\n"
    "v_exp_f32 v182, v182\n"
    "v_exp_f32 v135, v135\n"
    "v_exp_f32 v151, v151\n"
    "v_exp_f32 v167, v167\n"
    "v_exp_f32 v183, v183\n"
    "v_pk_add_f32 v[194:195], v[136:137], v[138:139]\n"
    "v_pk_add_f32 v[198:199], v[152:153], v[154:155]\n"
    "v_pk_add_f32 v[202:203], v[168:169], v[170:171]\n"
    "v_pk_add_f32 v[206:207], v[184:185], v[186:187]\n"
    "v_pk_add_f32 v[192:193], v[128:129], v[130:131]\n"
    "v_pk_add_f32 v[196:197], v[144:145], v[146:147]\n"
    "27: s_cbranch_execz 27f\n"
    "v_pk_add_f32 v[200:201], v[160:161], v[162:163]\n"
    "v_pk_add_f32 v[204:205], v[176:177], v[178:179]\n"
    "v_pk_add_f32 v[194:195], v[194:195], v[140:141]\n"
    "v_pk_add_f32 v[198:199], v[198:199], v[156:157]\n"
    "v_pk_add_f32 v[202:203], v[202:203], v[172:173]\n"
    "v_pk_add_f32 v[206:207], v[206:207], v[188:189]\n"
    "v_pk_add_f32 v[192:193], v[192:193], v[132:133]\n"
    "v_pk_add_f32 v[196:197], v[196:197], v[148:149]\n"
    "v_pk_add_f32 v[200:201], v[200:201], v[164:165]\n"
    "v_pk_add_f32 v[204:205], v[204:205], v[180:181]\n"
    "v_pk_add_f32 v[194:195], v[194:195], v[142:143]\n"
    "v_pk_add_f32 v[198:199], v[198:199], v[158:159]\n"
    "v_pk_add_f32 v[202:203], v[202:203], v[174:175]\n"
    "v_pk_add_f32 v[206:207], v[206:207], v[190:191]\n"
    "v_pk_add_f32 v[192:193], v[192:193], v[134:135]\n"
    "v_pk_add_f32 v[196:197], v[196:197], v[150:151]\n"
    "v_pk_add_f32 v[200:201], v[200:201], v[166:167]\n"
    "v_pk_add_f32 v[204:205], v[204:205], v[182:183]\n"
    "v_add_f32 v192, v192, v193\n"
    "v_add_f32 v196, v196, v197\n"
    "v_add_f32 v200, v200, v201\n"
    "v_add_f32 v204, v204, v205\n"
    "v_add_f32 v193, v194, v195\n"
    "v_add_f32 v197, v198, v199\n"
    "v_add_f32 v201, v202, v203\n"
    "v_add_f32 v205, v206, v207\n"
    "v_permlane32_swap_b32 v192, v193\n"
    "v_permlane32_swap_b32 v196, v197\n"
    "v_permlane32_swap_b32 v200, v201\n"
    "v_permlane32_swap_b32 v204, v205\n"
    "28: s_cbranch_execz 28f\n"
    "v_add_f32 v192, v192, v193\n"
    "v_add_f32 v196, v196, v197\n"
    "v_add_f32 v200, v200, v201\n"
    "v_add_f32 v204, v204, v205\n"
    "v_cmp_nge_f32 vcc, s58, v192\n"
    "s_or_b64 s[52:53], s[52:53], vcc\n"
    "v_cmp_nge_f32 vcc, s58, v196\n"
    "s_or_b64 s[52:53], s[52:53], vcc\n"
    "v_cmp_nge_f32 vcc, s58, v200\n"
    "s_or_b64 s[52:53], s[52:53], vcc\n"
    "v_cmp_nge_f32 vcc, s58, v204\n"
    "s_or_b64 s[52:53], s[52:53], vcc\n"
    "v_rcp_f32 v192, v192\n"
    "v_rcp_f32 v196, v196\n"
    "v_rcp_f32 v200, v200\n"
    "v_rcp_f32 v204, v204\n"
    "v_mov_b32 v193, v192\n"
    "v_mov_b32 v197, v196\n"
    "v_mov_b32 v201, v200\n"
    "v_mov_b32 v205, v204\n"
    "v_permlane32_swap_b32 v192, v193\n"
    "v_permlane32_swap_b32 v196, v197\n"
    "v_permlane32_swap_b32 v200, v201\n"
    "v_permlane32_swap_b32 v204, v205\n"
    "v_pk_mul_f32 v[128:129], v[128:129], v[192:193] op_sel_hi:[1,0]\n"
    "v_pk_mul_f32 v[144:145], v[144:145], v[196:197] op_sel_hi:[1,0]\n"
    "v_pk_mul_f32 v[160:161], v[160:161], v[200:201] op_sel_hi:[1,0]\n"
    "v_pk_mul_f32 v[176:177], v[176:177], v[204:205] op_sel_hi:[1,0]\n"
    "v_pk_mul_f32 v[130:131], v[130:131], v[192:193] op_sel_hi:[1,0]\n"
    "v_pk_mul_f32 v[146:147], v[146:147], v[196:197] op_sel_hi:[1,0]\n"
    "v_pk_mul_f32 v[162:163], v[162:163], v[200:201] op_sel_hi:[1,0]\n"
    "v_pk_mul_f32 v[178:179], v[178:179], v[204:205] op_sel_hi:[1,0]\n"
    "v_pk_mul_f32 v[132:133], v[132:133], v[192:193] op_sel_hi:[1,0]\n"
    "v_pk_mul_f32 v[148:149], v[148:149], v[196:197] op_sel_hi:[1,0]\n"
    "29: s_cbranch_execz 29f\n"
    "v_pk_mul_f32 v[164:165], v[164:165], v[200:201] op_sel_hi:[1,0]\n"
    "v_pk_mul_f32 v[180:181], v[180:181], v[204:205] op_sel_hi:[1,0]\n"
    "v_pk_mul_f32 v[134:135], v[134:135], v[192:193] op_sel_hi:[1,0]\n"
    "v_pk_mul_f32 v[150:151], v[150:151], v[196:197] op_sel_hi:[1,0]\n"
    "v_pk_mul_f32 v[166:167], v[166:167], v[200:201] op_sel_hi:[1,0]\n"
    "v_pk_mul_f32 v[182:183], v[182:183], v[204:205] op_sel_hi:[1,0]\n"
    "v_pk_mul_f32 v[136:137], v[136:137], v[192:193] op_sel:[0,1] op_sel_hi:[1,1]\n"
    "v_pk_mul_f32 v[152:153], v[152:153], v[196:197] op_sel:[0,1] op_sel_hi:[1,1]\n"
    "v_pk_mul_f32 v[168:169], v[168:169], v[200:201] op_sel:[0,1] op_sel_hi:[1,1]\n"
    "v_pk_mul_f32 v[184:185], v[184:185], v[204:205] op_sel:[0,1] op_sel_hi:[1,1]\n"
    "v_pk_mul_f32 v[138:139], v[138:139], v[192:193] op_sel:[0,1] op_sel_hi:[1,1]\n"
    "v_pk_mul_f32 v[154:155], v[154:155], v[196:197] op_sel:[0,1] op_sel_hi:[1,1]\n"
    "v_pk_mul_f32 v[170:171], v[170:171], v[200:201] op_sel:[0,1] op_sel_hi:[1,1]\n"
    "v_pk_mul_f32 v[186:187], v[186:187], v[204:205] op_sel:[0,1] op_sel_hi:[1,1]\n"
    "v_pk_mul_f32 v[140:141], v[140:141], v[192:193] op_sel:[0,1] op_sel_hi:[1,1]\n"
    "v_pk_mul_f32 v[156:157], v[156:157], v[196:197] op_sel:[0,1] op_sel_hi:[1,1]\n"
    "v_pk_mul_f32 v[172:173], v[172:173], v[200:201] op_sel:[0,1] op_sel_hi:[1,1]\n"
    "v_pk_mul_f32 v[188:189], v[188:189], v[204:205] op_sel:[0,1] op_sel_hi:[1,1]\n"
    "v_pk_mul_f32 v[142:143], v[142:143], v[192:193] op_sel:[0,1] op_sel_hi:[1,1]\n"
    "v_pk_mul_f32 v[158:159], v[158:159], v[196:197] op_sel:[0,1] op_sel_hi:[1,1]\n"
    "v_pk_mul_f32 v[174:175], v[174:175], v[200:201] op_sel:[0,1] op_sel_hi:[1,1]\n"
    "v_pk_mul_f32 v[190:191], v[190:191], v[204:205] op_sel:[0,1] op_sel_hi:[1,1]\n"
    "v_cvt_pk_bf16_f32 v128, v128, v129\n"
    "v_cvt_pk_bf16_f32 v144, v144, v145\n"
    "v_cvt_pk_bf16_f32 v160, v160, v161\n"
    "v_cvt_pk_bf16_f32 v176, v176, v177\n"
    "26: s_cbranch_execz 26f\n"
    "v_cvt_pk_bf16_f32 v129, v130, v131\n"
    "v_cvt_pk_bf16_f32 v145, v146, v147\n"
    "v_cvt_pk_bf16_f32 v161, v162, v163\n"
    "v_cvt_pk_bf16_f32 v177, v178, v179\n"
    "v_cvt_pk_bf16_f32 v130, v132, v133\n"
    "v_cvt_pk_bf16_f32 v146, v148, v149\n"
    "v_cvt_pk_bf16_f32 v162, v164, v165\n"
    "v_cvt_pk_bf16_f32 v178, v180, v181\n"
    "v_cvt_pk_bf16_f32 v131, v134, v135\n"
    "v_cvt_pk_bf16_f32 v147, v150, v151\n"
    "v_cvt_pk_bf16_f32 v163, v166, v167\n"
    "v_cvt_pk_bf16_f32 v179, v182, v183\n"
    "v_cvt_pk_bf16_f32 v132, v136, v137\n"
    "v_cvt_pk_bf16_f32 v148, v152, v153\n"
    "v_cvt_pk_bf16_f32 v164, v168, v169\n"
    "v_cvt_pk_bf16_f32 v180, v184, v185\n"
    "v_cvt_pk_bf16_f32 v133, v138, v139\n"
    "v_cvt_pk_bf16_f32 v149, v154, v155\n"
    "v_cvt_pk_bf16_f32 v165, v170, v171\n"
    "v_cvt_pk_bf16_f32 v181, v186, v187\n"
    "v_cvt_pk_bf16_f32 v134, v140, v141\n"
    "v_cvt_pk_bf16_f32 v150, v156, v157\n"
    "v_cvt_pk_bf16_f32 v166, v172, v173\n"
    "v_cvt_pk_bf16_f32 v182, v188, v189\n"
    "v_cvt_pk_bf16_f32 v135, v142, v143\n"
    "v_cvt_pk_bf16_f32 v151, v158, v159\n"
    "27: s_cbranch_execz 27f\n"
    "v_cvt_pk_bf16_f32 v167, v174, v175\n"
    "v_cvt_pk_bf16_f32 v183, v190, v191\n"
    "s_cmp_eq_u64 s[52:53], 0\n"
    "s_cbranch_scc1 J5\n"
    "s_getpc_b64 s[52:53]\n"
    "s_add_u32 s52, s52, 12\n"
    "s_addc_u32 s53, s53, 0\n"
    "s_branch PE_\n"
    "J5:\n"
    "v_mov_b64 v[0:1], 0\n"
    "v_mov_b64 v[2:3], 0\n"
    "v_mov_b64 v[4:5], 0\n"
    "v_mov_b64 v[6:7], 0\n"
    "v_mov_b64 v[8:9], 0\n"
    "v_mov_b64 v[10:11], 0\n"
    "v_mov_b64 v[12:13], 0\n"
    "v_mov_b64 v[14:15], 0\n"
    "v_mov_b64 v[16:17], 0\n"
    "v_mov_b64 v[18:19], 0\n"
    "v_mov_b64 v[20:21], 0\n"
    "v_mov_b64 v[22:23], 0\n"
    "v_mov_b64 v[24:25], 0\n"
    "v_mov_b64 v[26:27], 0\n"
    "v_mov_b64 v[28:29], 0\n"
    "v_mov_b64 v[30:31], 0\n"
    "v_mov_b64 v[88:89], 0\n"
    "v_mov_b64 v[90:91], 0\n"
    "v_mov_b64 v[92:93], 0\n"
    "v_mov_b64 v[94:95], 0\n"
    "v_and_b32 v192, 27, v106\n"
    "28: s_cbranch_execz 28f\n"
    "v_cmp_eq_u32 vcc, 16, v192\n"
    "v_mov_b32 v193, 0x3f803f80\n"
    "v_mov_b32 v250, 0xdddddddd\n"
    "v_cndmask_b32 v196, 0, v193, vcc\n"
    "v_mov_b32 v197, v196\n"
    "v_mov_b64 v[198:199], v[196:197]\n"
    "v_mov_b64 v[200:201], v[196:197]\n"
    "v_mov_b64 v[202:203], v[196:197]\n"
    "v_mov_b64 v[204:205], v[196:197]\n"
    "v_mov_b64 v[206:207], v[196:197]\n"
    "s_lshl_b32 s56, s29, 16\n"
    "v_add_u32 v104, s56, v105\n"
    "s_setprio 0\n"
    "s_mov_b32 s60, 0x100000\n"
    "W4:\n"
    "ds_read_b32 v112, v108 offset:16\n"
    "s_sub_u32 s60, s60, 1\n"
    "s_cbranch_scc1 W4x\n"
    "s_waitcnt lgkmcnt(0)\n"
    "v_readfirstlane_b32 s59, v112\n"
    "s_cmp_lt_u32 s59, 4\n"
    "s_cbranch_scc1 W4\n"
    "W4x:\n"
    "ds_write_b128 v117, v[224:227] offset:0\n"
    "ds_write_b128 v117, v[228:231] offset:1024\n"
    "ds_write_b128 v117, v[232:235] offset:8192\n"
    "ds_write_b128 v117, v[236:239] offset:9216\n"
    "ds_write_b128 v115, v[128:131] offset:0\n"
    "ds_write_b128 v116, v[132:135] offset:0\n"
    "ds_write_b128 v115, v[144:147] offset:8192\n"
    "ds_write_b128 v116, v[148:151] offset:8192\n"
    "ds_write_b128 v115, v[160:163] offset:16384\n"
    "29: s_cbranch_execz 29f\n"
    "ds_write_b128 v116, v[164:167] offset:16384\n"
    "ds_write_b128 v115, v[176:179] offset:24576\n"
    "ds_write_b128 v116, v[180:183] offset:24576\n"
    "s_mov_b64 exec, 1\n"
    "ds_add_u32 v108, v109 offset:36\n"
    "s_mov_b64 exec, -1\n"
    "s_mov_b32 s60, 0x100000\n"
    "W6:\n"
    "ds_read_b32 v112, v108 offset:36\n"
    "s_sub_u32 s60, s60, 1\n"
    "s_cbranch_scc1 W6x\n"
    "s_waitcnt lgkmcnt(0)\n"
    "v_readfirstlane_b32 s59, v112\n"
    "s_cmp_lt_u32 s59, 4\n"
    "s_cbranch_scc1 W6\n"
    "W6x:\n"
    "s_mov_b64 exec, s[50:51]\n"
    "ds_read_b128 v[32:35], v119 offset:0\n"
    "ds_read_b128 v[36:39], v119 offset:1024\n"
    "ds_read_b128 v[40:43], v119 offset:2048\n"
    "ds_read_b128 v[44:47], v119 offset:3072\n"
    "ds_read_b128 v[48:51], v119 offset:4096\n"
    "ds_read_b128 v[52:55], v119 offset:5120\n"
    "ds_read_b128 v[56:59], v119 offset:6144\n"
    "ds_read_b128 v[60:63], v119 offset:7168\n"
    "s_mov_b64 exec, -1\n"
    "ds_read_b64_tr_b16 v[128:129], v118 offset:0\n"
    "ds_read_b64_tr_b16 v[130:131], v118 offset:512\n"
    "ds_read_b64_tr_b16 v[132:133], v118 offset:1024\n"
    "ds_read_b64_tr_b16 v[134:135], v118 offset:1536\n"
    "ds_read_b64_tr_b16 v[136:137], v118 offset:2048\n"
    "ds_read_b64_tr_b16 v[138:139], v118 offset:2560\n"
    "ds_read_b64_tr_b16 v[140:141], v118 offset:3072\n"
    "26: s_cbranch_execz 26f\n"
    "ds_read_b64_tr_b16 v[142:143], v118 offset:3584\n"
    "s_waitcnt lgkmcnt(6)\n"
    "v_mfma_f32_32x32x16_bf16 v[208:223], v[128:131], v[32:35], 0\n"
    "ds_read_b64_tr_b16 v[144:145], v118 offset:4096\n"
    "ds_read_b64_tr_b16 v[146:147], v118 offset:4608\n"
    "s_waitcnt lgkmcnt(6)\n"
    "v_mfma_f32_32x32x16_bf16 v[208:223], v[132:135], v[36:39], v[208:223]\n"
    "ds_read_b64_tr_b16 v[148:149], v118 offset:5120\n"
    "ds_read_b64_tr_b16 v[150:151], v118 offset:5632\n"
    "s_waitcnt lgkmcnt(6)\n"
    "v_mfma_f32_32x32x16_bf16 v[208:223], v[136:139], v[40:43], v[208:223]\n"
    "ds_read_b64_tr_b16 v[152:153], v118 offset:6144\n"
    "ds_read_b64_tr_b16 v[154:155], v118 offset:6656\n"
    "s_waitcnt lgkmcnt(6)\n"
    "v_mfma_f32_32x32x16_bf16 v[208:223], v[140:143], v[44:47], v[208:223]\n"
    "ds_read_b64_tr_b16 v[156:157], v118 offset:7168\n"
    "ds_read_b64_tr_b16 v[158:159], v118 offset:7680\n"
    "s_waitcnt lgkmcnt(6)\n"
    "v_mfma_f32_32x32x16_bf16 v[208:223], v[144:147], v[48:51], v[208:223]\n"
    "s_waitcnt lgkmcnt(4)\n"
    "v_mfma_f32_32x32x16_bf16 v[208:223], v[148:151], v[52:55], v[208:223]\n"
    "s_waitcnt lgkmcnt(2)\n"
    "v_mfma_f32_32x32x16_bf16 v[208:223], v[152:155], v[56:59], v[208:223]\n"
    "s_waitcnt lgkmcnt(0)\n"
    "v_mfma_f32_32x32x16_bf16 v[208:223], v[156:159], v[60:63], v[208:223]\n"
    "s_mov_b64 exec, s[66:67]\n"
    "s_nop 9\n"
    "s_nop 0\n"
    "global_store_dwordx4 v120, v[208:211], s[64:65] offset:0 sc1\n"
    "global_store_dwordx4 v120, v[212:215], s[64:65] offset:544 sc1\n"
    "global_store_dwordx4 v120, v[216:219], s[64:65] offset:1088 sc1\n"
    "global_store_dwordx4 v120, v[220:223], s[64:65] offset:1632 sc1\n"
    "s_mov_b64 exec, -1\n"
    "31: s_cbranch_execz 31f\n"
    "B_:\n"
    "s_setprio 0\n"
    "s_mov_b64 exec, 1\n"
    "ds_add_u32 v108, v109 offset:20\n"
    "s_mov_b64 exec, -1\n"
    "s_lshl_b32 s56, s71, 3\n"
    "s_add_u32 s56, s56, 16777216\n"
    "s_add_u32 s52, s24, s56\n"
    "s_addc_u32 s53, s25, 0\n"
    "s_mov_b32 s54, 1\n"
    "s_mov_b32 s55, 1\n"
    "s_mov_b64 exec, s[54:55]\n"
    "global_store_dword v114, v113, s[52:53] sc1\n"
    "s_mov_b64 exec, -1\n"
    "s_lshl_b32 s56, s29, 16\n"
    "s_lshl_b32 s57, s28, 10\n"
    "s_add_u32 s58, s56, s57\n"
    "s_lshl_b32 s56, s36, 17\n"
    "s_add_u32 s56, s56, s58\n"
    "s_add_u32 s56, s56, 2097152\n"
    "s_add_u32 s50, s24, s56\n"
    "s_addc_u32 s51, s25, 0\n"
    "s_lshl2_add_u32 s56, s37, s28\n"
    "s_lshl1_add_u32 s70, s56, s29\n"
    "s_lshl_b32 s56, s56, 11\n"
    "s_lshl_b32 s57, s36, 16\n"
    "s_add_u32 s56, s56, s57\n"
    "s_add_u32 s48, s24, s56\n"
    "s_addc_u32 s49, s25, 0\n"
    "s_mov_b32 s60, 0x100000\n"
    "W9:\n"
    "ds_read_b32 v112, v108 offset:24\n"
    "s_sub_u32 s60, s60, 1\n"
    "s_cbranch_scc1 W9x\n"
    "s_waitcnt lgkmcnt(0)\n"
    "v_readfirstlane_b32 s59, v112\n"
    "s_cmp_lt_u32 s59, 4\n"
    "s_cbranch_scc1 W9\n"
    "W9x:\n"
    "s_mov_b32 s60, 0x100000\n"
    "W10:\n"
    "ds_read_b32 v112, v108 offset:20\n"
    "s_sub_u32 s60, s60, 1\n"
    "s_cbranch_scc1 W10x\n"
    "s_waitcnt lgkmcnt(0)\n"
    "v_readfirstlane_b32 s59, v112\n"
    "s_cmp_lt_u32 s59, 4\n"
    "s_cbranch_scc1 W10\n"
    "W10x:\n"
    "s_mov_b32 m0, s58\n"
    "s_mov_b32 s41, 16\n"
    "DL_11:\n"
    "global_load_lds_dwordx4 v105, s[50:51] sc1\n"
    "s_add_u32 m0, m0, 0x1000\n"
    "s_add_u32 s50, s50, 4096\n"
    "s_addc_u32 s51, s51, 0\n"
    "s_sub_u32 s41, s41, 1\n"
    "s_cmp_lg_u32 s41, 0\n"
    "s_cbranch_scc1 DL_11\n"
    "32: s_cbranch_execz 32f\n"
    "global_load_dwordx4 v[96:99], v105, s[48:49] sc1\n"
    "global_load_dwordx4 v[100:103], v105, s[48:49] offset:1024 sc1\n"
    "s_add_u32 s54, s42, 80\n"
    "s_addc_u32 s55, s43, 0\n"
    "s_mov_b64 exec, 0\n"
    "s_swappc_b64 s[44:45], s[54:55]\n"
    "s_mov_b64 exec, -1\n"
    "s_lshl1_add_u32 s56, s32, s33\n"
    "s_lshl4_add_u32 s56, s56, s34\n"
    "s_lshl_b32 s56, s56, 7\n"
    "s_add_u32 s56, s56, 17858560\n"
    "s_add_u32 s52, s24, s56\n"
    "s_addc_u32 s53, s25, 0\n"
    "s_lshl_b32 s56, s32, 8\n"
    "s_add_u32 s56, s56, s38\n"
    "s_lshl_b32 s56, s56, 11\n"
    "s_add_u32 s56, s56, 6291456\n"
    "s_add_u32 s48, s24, s56\n"
    "s_addc_u32 s49, s25, 0\n"
    "s_waitcnt vmcnt(0)\n"
    "s_mov_b64 exec, 1\n"
    "ds_add_u32 v108, v109 offset:32\n"
    "s_mov_b64 exec, -1\n"
    "s_cmp_lg_u32 s33, 0\n"
    "s_cbranch_scc1 J7\n"
    "global_store_dwordx4 v105, v[128:131], s[48:49] sc1\n"
    "global_store_dwordx4 v105, v[132:135], s[48:49] offset:1024 sc1\n"
    "s_add_u32 s48, s48, 131072\n"
    "s_addc_u32 s49, s49, 0\n"
    "global_store_dwordx4 v105, v[144:147], s[48:49] sc1\n"
    "global_store_dwordx4 v105, v[148:151], s[48:49] offset:1024 sc1\n"
    "s_add_u32 s48, s48, 131072\n"
    "s_addc_u32 s49, s49, 0\n"
    "global_store_dwordx4 v105, v[160:163], s[48:49] sc1\n"
    "global_store_dwordx4 v105, v[164:167], s[48:49] offset:1024 sc1\n"
    "s_add_u32 s48, s48, 131072\n"
    "s_addc_u32 s49, s49, 0\n"
    "global_store_dwordx4 v105, v[176:179], s[48:49] sc1\n"
    "33: s_cbranch_execz 33f\n"
    "global_store_dwordx4 v105, v[180:183], s[48:49] offset:1024 sc1\n"
    "s_setprio 3\n"
    "s_branch J8\n"
    "J7:\n"
    "s_mov_b64 exec, 1\n"
    "ds_add_rtn_u32 v112, v108, v109 offset:4\n"
    "v_mov_b64 v[240:241], s[82:83]\n"
    "v_mov_b32 v113, 0\n"
    "s_waitcnt lgkmcnt(0)\n"
    "v_cmp_eq_u32 vcc, 3, v112\n"
    "s_cbranch_vccz U12\n"
    "global_store_dwordx2 v113, v[240:241], s[52:53] sc1\n"
    "U12:\n"
    "s_mov_b64 exec, -1\n"
    "s_setprio 2\n"
    "J8:\n"
    "s_mov_b32 s60, 0x100000\n"
    "W13:\n"
    "ds_read_b32 v112, v108 offset:32\n"
    "s_sub_u32 s60, s60, 1\n"
    "s_cbranch_scc1 W13x\n"
    "s_waitcnt lgkmcnt(0)\n"
    "v_readfirstlane_b32 s59, v112\n"
    "s_cmp_lt_u32 s59, 4\n"
    "s_cbranch_scc1 W13\n"
    "W13x:\n"
    "s_branch AT_\n"
    "A0_:\n"
    "v_add_u32 v103, 0x10000, v103\n"
    "v_mov_b32 v0, 0\n"
    "v_lshlrev_b32 v1, 2, v106\n"
    "v_and_b32 v1, 0x7c, v1\n"
    "v_add_u32 v1, 163712, v1\n"
    "ds_write_b32 v1, v0\n"
    "s_add_u32 s54, s42, 0\n"
    "s_addc_u32 s55, s43, 0\n"
    "s_mov_b64 exec, 0\n"
    "s_swappc_b64 s[44:45], s[54:55]\n"
    "s_mov_b64 exec, -1\n"
    "s_lshl_b32 s56, s32, 8\n"
    "s_add_u32 s56, s56, s38\n"
    "s_lshl_b32 s57, s56, 11\n"
    "s_add_u32 s57, s57, 2097152\n"
    "s_add_u32 s48, s24, s57\n"
    "s_addc_u32 s49, s25, 0\n"
    "s_lshl_b32 s57, s56, 10\n"
    "s_add_u32 s50, s24, s57\n"
    "s_addc_u32 s51, s25, 0\n"
    "s_mov_b32 s52, 0x3eb8aa3b\n"
    "s_mov_b32 s53, 0x3eb8aa3b\n"
    "v_and_b32 v208, 0x33f, v105\n"
    "v_bfe_u32 v209, v106, 2, 1\n"
    "v_lshl_or_b32 v208, v209, 7, v208\n"
    "v_bfe_u32 v209, v106, 3, 1\n"
    "v_lshl_or_b32 v208, v209, 6, v208\n"
    "s_waitcnt vmcnt(5) lgkmcnt(0)\n"
    "s_barrier\n"
    "ds_read_b128 v[112:115], v99 offset:0\n"
    "ds_read_b64_tr_b16 v[64:65], v103 offset:0\n"
    "ds_read_b64_tr_b16 v[66:67], v103 offset:512\n"
    "ds_read_b64_tr_b16 v[68:69], v103 offset:256\n"
    "ds_read_b64_tr_b16 v[70:71], v103 offset:768\n"
    "ds_read_b64_tr_b16 v[72:73], v103 offset:8192\n"
    "ds_read_b64_tr_b16 v[74:75], v103 offset:8704\n"
    "ds_read_b64_tr_b16 v[76:77], v103 offset:8448\n"
    "ds_read_b64_tr_b16 v[78:79], v103 offset:8960\n"
    "s_waitcnt lgkmcnt(6)\n"
    "v_mfma_f32_32x32x16_bf16 v[0:15], v[64:67], v[112:115], 0\n"
    "ds_read_b128 v[116:119], v100 offset:0\n"
    "ds_read_b64_tr_b16 v[80:81], v103 offset:2048\n"
    "s_waitcnt lgkmcnt(6)\n"
    "v_mfma_f32_32x32x16_bf16 v[16:31], v[68:71], v[112:115], 0\n"
    "ds_read_b64_tr_b16 v[82:83], v103 offset:2560\n"
    "ds_read_b64_tr_b16 v[84:85], v103 offset:2304\n"
    "s_waitcnt lgkmcnt(6)\n"
    "v_mfma_f32_32x32x16_bf16 v[32:47], v[72:75], v[112:115], 0\n"
    "ds_read_b64_tr_b16 v[86:87], v103 offset:2816\n"
    "ds_read_b64_tr_b16 v[88:89], v103 offset:10240\n"
    "s_waitcnt lgkmcnt(6)\n"
    "v_mfma_f32_32x32x16_bf16 v[48:63], v[76:79], v[112:115], 0\n"
    "ds_read_b64_tr_b16 v[90:91], v103 offset:10752\n"
    "ds_read_b64_tr_b16 v[92:93], v103 offset:10496\n"
    "ds_read_b64_tr_b16 v[94:95], v103 offset:11008\n"
    "s_waitcnt lgkmcnt(6)\n"
    "v_mfma_f32_32x32x16_bf16 v[0:15], v[80:83], v[116:119], v[0:15]\n"
    "ds_read_b128 v[112:115], v101 offset:0\n"
    "ds_read_b64_tr_b16 v[64:65], v103 offset:4096\n"
    "s_waitcnt lgkmcnt(6)\n"
    "v_mfma_f32_32x32x16_bf16 v[16:31], v[84:87], v[116:119], v[16:31]\n"
    "ds_read_b64_tr_b16 v[66:67], v103 offset:4608\n"
    "ds_read_b64_tr_b16 v[68:69], v103 offset:4352\n"
    "s_waitcnt lgkmcnt(6)\n"
    "v_mfma_f32_32x32x16_bf16 v[32:47], v[88:91], v[116:119], v[32:47]\n"
    "ds_read_b64_tr_b16 v[70:71], v103 offset:4864\n"
    "ds_read_b64_tr_b16 v[72:73], v103 offset:12288\n"
    "s_waitcnt lgkmcnt(6)\n"
    "v_mfma_f32_32x32x16_bf16 v[48:63], v[92:95], v[116:119], v[48:63]\n"
    "ds_read_b64_tr_b16 v[74:75], v103 offset:12800\n"
    "ds_read_b64_tr_b16 v[76:77], v103 offset:12544\n"
    "ds_read_b64_tr_b16 v[78:79], v103 offset:13056\n"
    "s_waitcnt lgkmcnt(6)\n"
    "v_mfma_f32_32x32x16_bf16 v[0:15], v[64:67], v[112:115], v[0:15]\n"
    "ds_read_b128 v[116:119], v102 offset:0\n"
    "ds_read_b64_tr_b16 v[80:81], v103 offset:6144\n"
    "s_waitcnt lgkmcnt(6)\n"
    "v_mfma_f32_32x32x16_bf16 v[16:31], v[68:71], v[112:115], v[16:31]\n"
    "ds_read_b64_tr_b16 v[82:83], v103 offset:6656\n"
    "ds_read_b64_tr_b16 v[84:85], v103 offset:6400\n"
    "s_waitcnt lgkmcnt(6)\n"
    "v_mfma_f32_32x32x16_bf16 v[32:47], v[72:75], v[112:115], v[32:47]\n"
    "ds_read_b64_tr_b16 v[86:87], v103 offset:6912\n"
    "ds_read_b64_tr_b16 v[88:89], v103 offset:14336\n"
    "s_waitcnt lgkmcnt(6)\n"
    "v_mfma_f32_32x32x16_bf16 v[48:63], v[76:79], v[112:115], v[48:63]\n"
    "ds_read_b64_tr_b16 v[90:91], v103 offset:14848\n"
    "ds_read_b64_tr_b16 v[92:93], v103 offset:14592\n"
    "ds_read_b64_tr_b16 v[94:95], v103 offset:15104\n"
    "s_waitcnt vmcnt(0) lgkmcnt(0)\n"
    "s_barrier\n"
    "v_mfma_f32_32x32x16_bf16 v[0:15], v[80:83], v[116:119], v[0:15]\n"
    "ds_read_b128 v[112:115], v99 offset:16384\n"
    "ds_read_b64_tr_b16 v[64:65], v103 offset:16384\n"
    "v_mfma_f32_32x32x16_bf16 v[16:31], v[84:87], v[116:119], v[16:31]\n"
    "ds_read_b64_tr_b16 v[66:67], v103 offset:16896\n"
    "ds_read_b64_tr_b16 v[68:69], v103 offset:16640\n"
    "v_mfma_f32_32x32x16_bf16 v[32:47], v[88:91], v[116:119], v[32:47]\n"
    "ds_read_b64_tr_b16 v[70:71], v103 offset:17152\n"
    "ds_read_b64_tr_b16 v[72:73], v103 offset:24576\n"
    "v_mfma_f32_32x32x16_bf16 v[48:63], v[92:95], v[116:119], v[48:63]\n"
    "ds_read_b64_tr_b16 v[74:75], v103 offset:25088\n"
    "ds_read_b64_tr_b16 v[76:77], v103 offset:24832\n"
    "ds_read_b64_tr_b16 v[78:79], v103 offset:25344\n"
    "s_waitcnt lgkmcnt(6)\n"
    "v_mfma_f32_32x32x16_bf16 v[0:15], v[64:67], v[112:115], v[0:15]\n"
    "ds_read_b128 v[116:119], v100 offset:16384\n"
    "ds_read_b64_tr_b16 v[80:81], v103 offset:18432\n"
    "s_waitcnt lgkmcnt(6)\n"
    "v_mfma_f32_32x32x16_bf16 v[16:31], v[68:71], v[112:115], v[16:31]\n"
    "ds_read_b64_tr_b16 v[82:83], v103 offset:18944\n"
    "ds_read_b64_tr_b16 v[84:85], v103 offset:18688\n"
    "s_waitcnt lgkmcnt(6)\n"
    "v_mfma_f32_32x32x16_bf16 v[32:47], v[72:75], v[112:115], v[32:47]\n"
    "ds_read_b64_tr_b16 v[86:87], v103 offset:19200\n"
    "ds_read_b64_tr_b16 v[88:89], v103 offset:26624\n"
    "s_waitcnt lgkmcnt(6)\n"
    "v_mfma_f32_32x32x16_bf16 v[48:63], v[76:79], v[112:115], v[48:63]\n"
    "ds_read_b64_tr_b16 v[90:91], v103 offset:27136\n"
    "ds_read_b64_tr_b16 v[92:93], v103 offset:26880\n"
    "ds_read_b64_tr_b16 v[94:95], v103 offset:27392\n"
    "s_waitcnt lgkmcnt(6)\n"
    "v_mfma_f32_32x32x16_bf16 v[0:15], v[80:83], v[116:119], v[0:15]\n"
    "ds_read_b128 v[112:115], v101 offset:16384\n"
    "ds_read_b64_tr_b16 v[64:65], v103 offset:20480\n"
    "s_waitcnt lgkmcnt(6)\n"
    "v_mfma_f32_32x32x16_bf16 v[16:31], v[84:87], v[116:119], v[16:31]\n"
    "ds_read_b64_tr_b16 v[66:67], v103 offset:20992\n"
    "ds_read_b64_tr_b16 v[68:69], v103 offset:20736\n"
    "s_waitcnt lgkmcnt(6)\n"
    "v_mfma_f32_32x32x16_bf16 v[32:47], v[88:91], v[116:119], v[32:47]\n"
    "ds_read_b64_tr_b16 v[70:71], v103 offset:21248\n"
    "ds_read_b64_tr_b16 v[72:73], v103 offset:28672\n"
    "s_waitcnt lgkmcnt(6)\n"
    "v_mfma_f32_32x32x16_bf16 v[48:63], v[92:95], v[116:119], v[48:63]\n"
    "ds_read_b64_tr_b16 v[74:75], v103 offset:29184\n"
    "ds_read_b64_tr_b16 v[76:77], v103 offset:28928\n"
    "ds_read_b64_tr_b16 v[78:79], v103 offset:29440\n"
    "s_waitcnt lgkmcnt(6)\n"
    "v_mfma_f32_32x32x16_bf16 v[0:15], v[64:67], v[112:115], v[0:15]\n"
    "ds_read_b128 v[116:119], v102 offset:16384\n"
    "ds_read_b64_tr_b16 v[80:81], v103 offset:22528\n"
    "s_waitcnt lgkmcnt(6)\n"
    "v_mfma_f32_32x32x16_bf16 v[16:31], v[68:71], v[112:115], v[16:31]\n"
    "ds_read_b64_tr_b16 v[82:83], v103 offset:23040\n"
    "ds_read_b64_tr_b16 v[84:85], v103 offset:22784\n"
    "s_waitcnt lgkmcnt(6)\n"
    "v_mfma_f32_32x32x16_bf16 v[32:47], v[72:75], v[112:115], v[32:47]\n"
    "ds_read_b64_tr_b16 v[86:87], v103 offset:23296\n"
    "ds_read_b64_tr_b16 v[88:89], v103 offset:30720\n"
    "s_waitcnt lgkmcnt(6)\n"
    "v_mfma_f32_32x32x16_bf16 v[48:63], v[76:79], v[112:115], v[48:63]\n"
    "ds_read_b64_tr_b16 v[90:91], v103 offset:31232\n"
    "ds_read_b64_tr_b16 v[92:93], v103 offset:30976\n"
    "ds_read_b64_tr_b16 v[94:95], v103 offset:31488\n"
    "s_waitcnt lgkmcnt(0)\n"
    "s_barrier\n"
    "v_mfma_f32_32x32x16_bf16 v[0:15], v[80:83], v[116:119], v[0:15]\n"
    "ds_read_b128 v[112:115], v99 offset:32768\n"
    "ds_read_b64_tr_b16 v[64:65], v103 offset:32768\n"
    "v_mfma_f32_32x32x16_bf16 v[16:31], v[84:87], v[116:119], v[16:31]\n"
    "ds_read_b64_tr_b16 v[66:67], v103 offset:33280\n"
    "ds_read_b64_tr_b16 v[68:69], v103 offset:33024\n"
    "v_mfma_f32_32x32x16_bf16 v[32:47], v[88:91], v[116:119], v[32:47]\n"
    "ds_read_b64_tr_b16 v[70:71], v103 offset:33536\n"
    "ds_read_b64_tr_b16 v[72:73], v103 offset:40960\n"
    "v_mfma_f32_32x32x16_bf16 v[48:63], v[92:95], v[116:119], v[48:63]\n"
    "ds_read_b64_tr_b16 v[74:75], v103 offset:41472\n"
    "ds_read_b64_tr_b16 v[76:77], v103 offset:41216\n"
    "ds_read_b64_tr_b16 v[78:79], v103 offset:41728\n"
    "s_waitcnt lgkmcnt(6)\n"
    "v_mfma_f32_32x32x16_bf16 v[0:15], v[64:67], v[112:115], v[0:15]\n"
    "ds_read_b128 v[116:119], v100 offset:32768\n"
    "ds_read_b64_tr_b16 v[80:81], v103 offset:34816\n"
    "s_waitcnt lgkmcnt(6)\n"
    "v_mfma_f32_32x32x16_bf16 v[16:31], v[68:71], v[112:115], v[16:31]\n"
    "ds_read_b64_tr_b16 v[82:83], v103 offset:35328\n"
    "ds_read_b64_tr_b16 v[84:85], v103 offset:35072\n"
    "s_waitcnt lgkmcnt(6)\n"
    "v_mfma_f32_32x32x16_bf16 v[32:47], v[72:75], v[112:115], v[32:47]\n"
    "ds_read_b64_tr_b16 v[86:87], v103 offset:35584\n"
    "ds_read_b64_tr_b16 v[88:89], v103 offset:43008\n"
    "s_waitcnt lgkmcnt(6)\n"
    "v_mfma_f32_32x32x16_bf16 v[48:63], v[76:79], v[112:115], v[48:63]\n"
    "ds_read_b64_tr_b16 v[90:91], v103 offset:43520\n"
    "ds_read_b64_tr_b16 v[92:93], v103 offset:43264\n"
    "ds_read_b64_tr_b16 v[94:95], v103 offset:43776\n"
    "s_waitcnt lgkmcnt(6)\n"
    "v_mfma_f32_32x32x16_bf16 v[0:15], v[80:83], v[116:119], v[0:15]\n"
    "ds_read_b128 v[112:115], v101 offset:32768\n"
    "ds_read_b64_tr_b16 v[64:65], v103 offset:36864\n"
    "s_waitcnt lgkmcnt(6)\n"
    "v_mfma_f32_32x32x16_bf16 v[16:31], v[84:87], v[116:119], v[16:31]\n"
    "ds_read_b64_tr_b16 v[66:67], v103 offset:37376\n"
    "ds_read_b64_tr_b16 v[68:69], v103 offset:37120\n"
    "s_waitcnt lgkmcnt(6)\n"
    "v_mfma_f32_32x32x16_bf16 v[32:47], v[88:91], v[116:119], v[32:47]\n"
    "ds_read_b64_tr_b16 v[70:71], v103 offset:37632\n"
    "ds_read_b64_tr_b16 v[72:73], v103 offset:45056\n"
    "s_waitcnt lgkmcnt(6)\n"
    "v_mfma_f32_32x32x16_bf16 v[48:63], v[92:95], v[116:119], v[48:63]\n"
    "ds_read_b64_tr_b16 v[74:75], v103 offset:45568\n"
    "ds_read_b64_tr_b16 v[76:77], v103 offset:45312\n"
    "ds_read_b64_tr_b16 v[78:79], v103 offset:45824\n"
    "s_waitcnt lgkmcnt(6)\n"
    "v_mfma_f32_32x32x16_bf16 v[0:15], v[64:67], v[112:115], v[0:15]\n"
    "ds_read_b128 v[116:119], v102 offset:32768\n"
    "ds_read_b64_tr_b16 v[80:81], v103 offset:38912\n"
    "s_waitcnt lgkmcnt(6)\n"
    "v_mfma_f32_32x32x16_bf16 v[16:31], v[68:71], v[112:115], v[16:31]\n"
    "ds_read_b64_tr_b16 v[82:83], v103 offset:39424\n"
    "ds_read_b64_tr_b16 v[84:85], v103 offset:39168\n"
    "s_waitcnt lgkmcnt(6)\n"
    "v_mfma_f32_32x32x16_bf16 v[32:47], v[72:75], v[112:115], v[32:47]\n"
    "ds_read_b64_tr_b16 v[86:87], v103 offset:39680\n"
    "ds_read_b64_tr_b16 v[88:89], v103 offset:47104\n"
    "s_waitcnt lgkmcnt(6)\n"
    "v_mfma_f32_32x32x16_bf16 v[48:63], v[76:79], v[112:115], v[48:63]\n"
    "ds_read_b64_tr_b16 v[90:91], v103 offset:47616\n"
    "ds_read_b64_tr_b16 v[92:93], v103 offset:47360\n"
    "ds_read_b64_tr_b16 v[94:95], v103 offset:47872\n"
    "s_waitcnt lgkmcnt(0)\n"
    "s_barrier\n"
    "v_mfma_f32_32x32x16_bf16 v[0:15], v[80:83], v[116:119], v[0:15]\n"
    "ds_read_b128 v[112:115], v99 offset:49152\n"
    "ds_read_b64_tr_b16 v[64:65], v103 offset:49152\n"
    "v_mfma_f32_32x32x16_bf16 v[16:31], v[84:87], v[116:119], v[16:31]\n"
    "ds_read_b64_tr_b16 v[66:67], v103 offset:49664\n"
    "ds_read_b64_tr_b16 v[68:69], v103 offset:49408\n"
    "v_mfma_f32_32x32x16_bf16 v[32:47], v[88:91], v[116:119], v[32:47]\n"
    "ds_read_b64_tr_b16 v[70:71], v103 offset:49920\n"
    "ds_read_b64_tr_b16 v[72:73], v103 offset:57344\n"
    "v_mfma_f32_32x32x16_bf16 v[48:63], v[92:95], v[116:119], v[48:63]\n"
    "ds_read_b64_tr_b16 v[74:75], v103 offset:57856\n"
    "ds_read_b64_tr_b16 v[76:77], v103 offset:57600\n"
    "ds_read_b64_tr_b16 v[78:79], v103 offset:58112\n"
    "s_waitcnt lgkmcnt(6)\n"
    "v_mfma_f32_32x32x16_bf16 v[0:15], v[64:67], v[112:115], v[0:15]\n"
    "ds_read_b128 v[116:119], v100 offset:49152\n"
    "ds_read_b64_tr_b16 v[80:81], v103 offset:51200\n"
    "s_waitcnt lgkmcnt(6)\n"
    "v_mfma_f32_32x32x16_bf16 v[16:31], v[68:71], v[112:115], v[16:31]\n"
    "ds_read_b64_tr_b16 v[82:83], v103 offset:51712\n"
    "ds_read_b64_tr_b16 v[84:85], v103 offset:51456\n"
    "s_waitcnt lgkmcnt(6)\n"
    "v_mfma_f32_32x32x16_bf16 v[32:47], v[72:75], v[112:115], v[32:47]\n"
    "ds_read_b64_tr_b16 v[86:87], v103 offset:51968\n"
    "ds_read_b64_tr_b16 v[88:89], v103 offset:59392\n"
    "s_waitcnt lgkmcnt(6)\n"
    "v_mfma_f32_32x32x16_bf16 v[48:63], v[76:79], v[112:115], v[48:63]\n"
    "ds_read_b64_tr_b16 v[90:91], v103 offset:59904\n"
    "ds_read_b64_tr_b16 v[92:93], v103 offset:59648\n"
    "ds_read_b64_tr_b16 v[94:95], v103 offset:60160\n"
    "s_waitcnt lgkmcnt(6)\n"
    "v_mfma_f32_32x32x16_bf16 v[0:15], v[80:83], v[116:119], v[0:15]\n"
    "ds_read_b128 v[112:115], v101 offset:49152\n"
    "ds_read_b64_tr_b16 v[64:65], v103 offset:53248\n"
    "s_waitcnt lgkmcnt(6)\n"
    "v_mfma_f32_32x32x16_bf16 v[16:31], v[84:87], v[116:119], v[16:31]\n"
    "ds_read_b64_tr_b16 v[66:67], v103 offset:53760\n"
    "ds_read_b64_tr_b16 v[68:69], v103 offset:53504\n"
    "s_waitcnt lgkmcnt(6)\n"
    "v_mfma_f32_32x32x16_bf16 v[32:47], v[88:91], v[116:119], v[32:47]\n"
    "ds_read_b64_tr_b16 v[70:71], v103 offset:54016\n"
    "ds_read_b64_tr_b16 v[72:73], v103 offset:61440\n"
    "s_waitcnt lgkmcnt(6)\n"
    "v_mfma_f32_32x32x16_bf16 v[48:63], v[92:95], v[116:119], v[48:63]\n"
    "ds_read_b64_tr_b16 v[74:75], v103 offset:61952\n"
    "ds_read_b64_tr_b16 v[76:77], v103 offset:61696\n"
    "ds_read_b64_tr_b16 v[78:79], v103 offset:62208\n"
    "s_waitcnt lgkmcnt(6)\n"
    "v_mfma_f32_32x32x16_bf16 v[0:15], v[64:67], v[112:115], v[0:15]\n"
    "ds_read_b128 v[116:119], v102 offset:49152\n"
    "ds_read_b64_tr_b16 v[80:81], v103 offset:55296\n"
    "s_waitcnt lgkmcnt(6)\n"
    "v_mfma_f32_32x32x16_bf16 v[16:31], v[68:71], v[112:115], v[16:31]\n"
    "ds_read_b64_tr_b16 v[82:83], v103 offset:55808\n"
    "ds_read_b64_tr_b16 v[84:85], v103 offset:55552\n"
    "s_waitcnt lgkmcnt(6)\n"
    "v_mfma_f32_32x32x16_bf16 v[32:47], v[72:75], v[112:115], v[32:47]\n"
    "ds_read_b64_tr_b16 v[86:87], v103 offset:56064\n"
    "ds_read_b64_tr_b16 v[88:89], v103 offset:63488\n"
    "s_waitcnt lgkmcnt(6)\n"
    "v_mfma_f32_32x32x16_bf16 v[48:63], v[76:79], v[112:115], v[48:63]\n"
    "ds_read_b64_tr_b16 v[90:91], v103 offset:64000\n"
    "ds_read_b64_tr_b16 v[92:93], v103 offset:63744\n"
    "ds_read_b64_tr_b16 v[94:95], v103 offset:64256\n"
    "s_waitcnt lgkmcnt(0)\n"
    "s_barrier\n"
    "v_mfma_f32_32x32x16_bf16 v[0:15], v[80:83], v[116:119], v[0:15]\n"
    "ds_read_b128 v[112:115], v99 offset:0\n"
    "ds_read_b64_tr_b16 v[64:65], v103 offset:0\n"
    "v_mfma_f32_32x32x16_bf16 v[16:31], v[84:87], v[116:119], v[16:31]\n"
    "ds_read_b64_tr_b16 v[66:67], v103 offset:512\n"
    "ds_read_b64_tr_b16 v[68:69], v103 offset:256\n"
    "v_mfma_f32_32x32x16_bf16 v[32:47], v[88:91], v[116:119], v[32:47]\n"
    "ds_read_b64_tr_b16 v[70:71], v103 offset:768\n"
    "ds_read_b64_tr_b16 v[72:73], v103 offset:8192\n"
    "v_mfma_f32_32x32x16_bf16 v[48:63], v[92:95], v[116:119], v[48:63]\n"
    "ds_read_b64_tr_b16 v[74:75], v103 offset:8704\n"
    "ds_read_b64_tr_b16 v[76:77], v103 offset:8448\n"
    "ds_read_b64_tr_b16 v[78:79], v103 offset:8960\n"
    "s_waitcnt lgkmcnt(6)\n"
    "v_mfma_f32_32x32x16_bf16 v[0:15], v[64:67], v[112:115], v[0:15]\n"
    "ds_read_b128 v[116:119], v100 offset:0\n"
    "ds_read_b64_tr_b16 v[80:81], v103 offset:2048\n"
    "s_waitcnt lgkmcnt(6)\n"
    "v_mfma_f32_32x32x16_bf16 v[16:31], v[68:71], v[112:115], v[16:31]\n"
    "ds_read_b64_tr_b16 v[82:83], v103 offset:2560\n"
    "ds_read_b64_tr_b16 v[84:85], v103 offset:2304\n"
    "s_waitcnt lgkmcnt(6)\n"
    "v_mfma_f32_32x32x16_bf16 v[32:47], v[72:75], v[112:115], v[32:47]\n"
    "ds_read_b64_tr_b16 v[86:87], v103 offset:2816\n"
    "ds_read_b64_tr_b16 v[88:89], v103 offset:10240\n"
    "s_waitcnt lgkmcnt(6)\n"
    "v_mfma_f32_32x32x16_bf16 v[48:63], v[76:79], v[112:115], v[48:63]\n"
    "ds_read_b64_tr_b16 v[90:91], v103 offset:10752\n"
    "ds_read_b64_tr_b16 v[92:93], v103 offset:10496\n"
    "ds_read_b64_tr_b16 v[94:95], v103 offset:11008\n"
    "s_waitcnt lgkmcnt(6)\n"
    "v_mfma_f32_32x32x16_bf16 v[0:15], v[80:83], v[116:119], v[0:15]\n"
    "ds_read_b128 v[112:115], v101 offset:0\n"
    "ds_read_b64_tr_b16 v[64:65], v103 offset:4096\n"
    "s_waitcnt lgkmcnt(6)\n"
    "v_mfma_f32_32x32x16_bf16 v[16:31], v[84:87], v[116:119], v[16:31]\n"
    "ds_read_b64_tr_b16 v[66:67], v103 offset:4608\n"
    "ds_read_b64_tr_b16 v[68:69], v103 offset:4352\n"
    "s_waitcnt lgkmcnt(6)\n"
    "v_mfma_f32_32x32x16_bf16 v[32:47], v[88:91], v[116:119], v[32:47]\n"
    "ds_read_b64_tr_b16 v[70:71], v103 offset:4864\n"
    "ds_read_b64_tr_b16 v[72:73], v103 offset:12288\n"
    "s_waitcnt lgkmcnt(6)\n"
    "v_mfma_f32_32x32x16_bf16 v[48:63], v[92:95], v[116:119], v[48:63]\n"
    "ds_read_b64_tr_b16 v[74:75], v103 offset:12800\n"
    "ds_read_b64_tr_b16 v[76:77], v103 offset:12544\n"
    "ds_read_b64_tr_b16 v[78:79], v103 offset:13056\n"
    "s_waitcnt lgkmcnt(6)\n"
    "v_mfma_f32_32x32x16_bf16 v[0:15], v[64:67], v[112:115], v[0:15]\n"
    "ds_read_b128 v[116:119], v102 offset:0\n"
    "ds_read_b64_tr_b16 v[80:81], v103 offset:6144\n"
    "s_waitcnt lgkmcnt(6)\n"
    "v_mfma_f32_32x32x16_bf16 v[16:31], v[68:71], v[112:115], v[16:31]\n"
    "ds_read_b64_tr_b16 v[82:83], v103 offset:6656\n"
    "ds_read_b64_tr_b16 v[84:85], v103 offset:6400\n"
    "s_waitcnt lgkmcnt(6)\n"
    "v_mfma_f32_32x32x16_bf16 v[32:47], v[72:75], v[112:115], v[32:47]\n"
    "ds_read_b64_tr_b16 v[86:87], v103 offset:6912\n"
    "ds_read_b64_tr_b16 v[88:89], v103 offset:14336\n"
    "s_waitcnt lgkmcnt(6)\n"
    "v_mfma_f32_32x32x16_bf16 v[48:63], v[76:79], v[112:115], v[48:63]\n"
    "ds_read_b64_tr_b16 v[90:91], v103 offset:14848\n"
    "ds_read_b64_tr_b16 v[92:93], v103 offset:14592\n"
    "ds_read_b64_tr_b16 v[94:95], v103 offset:15104\n"
    "s_waitcnt lgkmcnt(0)\n"
    "s_barrier\n"
    "v_mfma_f32_32x32x16_bf16 v[0:15], v[80:83], v[116:119], v[0:15]\n"
    "ds_read_b128 v[112:115], v99 offset:16384\n"
    "ds_read_b64_tr_b16 v[64:65], v103 offset:16384\n"
    "v_mfma_f32_32x32x16_bf16 v[16:31], v[84:87], v[116:119], v[16:31]\n"
    "ds_read_b64_tr_b16 v[66:67], v103 offset:16896\n"
    "ds_read_b64_tr_b16 v[68:69], v103 offset:16640\n"
    "v_mfma_f32_32x32x16_bf16 v[32:47], v[88:91], v[116:119], v[32:47]\n"
    "ds_read_b64_tr_b16 v[70:71], v103 offset:17152\n"
    "ds_read_b64_tr_b16 v[72:73], v103 offset:24576\n"
    "v_mfma_f32_32x32x16_bf16 v[48:63], v[92:95], v[116:119], v[48:63]\n"
    "ds_read_b64_tr_b16 v[74:75], v103 offset:25088\n"
    "ds_read_b64_tr_b16 v[76:77], v103 offset:24832\n"
    "ds_read_b64_tr_b16 v[78:79], v103 offset:25344\n"
    "s_waitcnt lgkmcnt(6)\n"
    "v_mfma_f32_32x32x16_bf16 v[0:15], v[64:67], v[112:115], v[0:15]\n"
    "ds_read_b128 v[116:119], v100 offset:16384\n"
    "ds_read_b64_tr_b16 v[80:81], v103 offset:18432\n"
    "s_waitcnt lgkmcnt(6)\n"
    "v_mfma_f32_32x32x16_bf16 v[16:31], v[68:71], v[112:115], v[16:31]\n"
    "ds_read_b64_tr_b16 v[82:83], v103 offset:18944\n"
    "ds_read_b64_tr_b16 v[84:85], v103 offset:18688\n"
    "s_waitcnt lgkmcnt(6)\n"
    "v_mfma_f32_32x32x16_bf16 v[32:47], v[72:75], v[112:115], v[32:47]\n"
    "ds_read_b64_tr_b16 v[86:87], v103 offset:19200\n"
    "ds_read_b64_tr_b16 v[88:89], v103 offset:26624\n"
    "s_waitcnt lgkmcnt(6)\n"
    "v_mfma_f32_32x32x16_bf16 v[48:63], v[76:79], v[112:115], v[48:63]\n"
    "ds_read_b64_tr_b16 v[90:91], v103 offset:27136\n"
    "ds_read_b64_tr_b16 v[92:93], v103 offset:26880\n"
    "ds_read_b64_tr_b16 v[94:95], v103 offset:27392\n"
    "s_waitcnt lgkmcnt(6)\n"
    "v_mfma_f32_32x32x16_bf16 v[0:15], v[80:83], v[116:119], v[0:15]\n"
    "ds_read_b128 v[112:115], v101 offset:16384\n"
    "ds_read_b64_tr_b16 v[64:65], v103 offset:20480\n"
    "s_waitcnt lgkmcnt(6)\n"
    "v_mfma_f32_32x32x16_bf16 v[16:31], v[84:87], v[116:119], v[16:31]\n"
    "ds_read_b64_tr_b16 v[66:67], v103 offset:20992\n"
    "ds_read_b64_tr_b16 v[68:69], v103 offset:20736\n"
    "s_waitcnt lgkmcnt(6)\n"
    "v_mfma_f32_32x32x16_bf16 v[32:47], v[88:91], v[116:119], v[32:47]\n"
    "ds_read_b64_tr_b16 v[70:71], v103 offset:21248\n"
    "ds_read_b64_tr_b16 v[72:73], v103 offset:28672\n"
    "s_waitcnt lgkmcnt(6)\n"
    "v_mfma_f32_32x32x16_bf16 v[48:63], v[92:95], v[116:119], v[48:63]\n"
    "ds_read_b64_tr_b16 v[74:75], v103 offset:29184\n"
    "ds_read_b64_tr_b16 v[76:77], v103 offset:28928\n"
    "ds_read_b64_tr_b16 v[78:79], v103 offset:29440\n"
    "s_waitcnt lgkmcnt(6)\n"
    "v_mfma_f32_32x32x16_bf16 v[0:15], v[64:67], v[112:115], v[0:15]\n"
    "ds_read_b128 v[116:119], v102 offset:16384\n"
    "ds_read_b64_tr_b16 v[80:81], v103 offset:22528\n"
    "s_waitcnt lgkmcnt(6)\n"
    "v_mfma_f32_32x32x16_bf16 v[16:31], v[68:71], v[112:115], v[16:31]\n"
    "ds_read_b64_tr_b16 v[82:83], v103 offset:23040\n"
    "ds_read_b64_tr_b16 v[84:85], v103 offset:22784\n"
    "s_waitcnt lgkmcnt(6)\n"
    "v_mfma_f32_32x32x16_bf16 v[32:47], v[72:75], v[112:115], v[32:47]\n"
    "ds_read_b64_tr_b16 v[86:87], v103 offset:23296\n"
    "ds_read_b64_tr_b16 v[88:89], v103 offset:30720\n"
    "s_waitcnt lgkmcnt(6)\n"
    "v_mfma_f32_32x32x16_bf16 v[48:63], v[76:79], v[112:115], v[48:63]\n"
    "ds_read_b64_tr_b16 v[90:91], v103 offset:31232\n"
    "ds_read_b64_tr_b16 v[92:93], v103 offset:30976\n"
    "ds_read_b64_tr_b16 v[94:95], v103 offset:31488\n"
    "s_waitcnt lgkmcnt(0)\n"
    "s_barrier\n"
    "v_mfma_f32_32x32x16_bf16 v[0:15], v[80:83], v[116:119], v[0:15]\n"
    "ds_read_b128 v[112:115], v99 offset:32768\n"
    "ds_read_b64_tr_b16 v[64:65], v103 offset:32768\n"
    "v_mfma_f32_32x32x16_bf16 v[16:31], v[84:87], v[116:119], v[16:31]\n"
    "ds_read_b64_tr_b16 v[66:67], v103 offset:33280\n"
    "ds_read_b64_tr_b16 v[68:69], v103 offset:33024\n"
    "v_mfma_f32_32x32x16_bf16 v[32:47], v[88:91], v[116:119], v[32:47]\n"
    "ds_read_b64_tr_b16 v[70:71], v103 offset:33536\n"
    "ds_read_b64_tr_b16 v[72:73], v103 offset:40960\n"
    "v_mfma_f32_32x32x16_bf16 v[48:63], v[92:95], v[116:119], v[48:63]\n"
    "ds_read_b64_tr_b16 v[74:75], v103 offset:41472\n"
    "ds_read_b64_tr_b16 v[76:77], v103 offset:41216\n"
    "ds_read_b64_tr_b16 v[78:79], v103 offset:41728\n"
    "s_waitcnt lgkmcnt(6)\n"
    "v_mfma_f32_32x32x16_bf16 v[0:15], v[64:67], v[112:115], v[0:15]\n"
    "ds_read_b128 v[116:119], v100 offset:32768\n"
    "ds_read_b64_tr_b16 v[80:81], v103 offset:34816\n"
    "s_waitcnt lgkmcnt(6)\n"
    "v_mfma_f32_32x32x16_bf16 v[16:31], v[68:71], v[112:115], v[16:31]\n"
    "ds_read_b64_tr_b16 v[82:83], v103 offset:35328\n"
    "ds_read_b64_tr_b16 v[84:85], v103 offset:35072\n"
    "s_waitcnt lgkmcnt(6)\n"
    "v_mfma_f32_32x32x16_bf16 v[32:47], v[72:75], v[112:115], v[32:47]\n"
    "ds_read_b64_tr_b16 v[86:87], v103 offset:35584\n"
    "ds_read_b64_tr_b16 v[88:89], v103 offset:43008\n"
    "s_waitcnt lgkmcnt(6)\n"
    "v_mfma_f32_32x32x16_bf16 v[48:63], v[76:79], v[112:115], v[48:63]\n"
    "ds_read_b64_tr_b16 v[90:91], v103 offset:43520\n"
    "ds_read_b64_tr_b16 v[92:93], v103 offset:43264\n"
    "ds_read_b64_tr_b16 v[94:95], v103 offset:43776\n"
    "s_waitcnt lgkmcnt(6)\n"
    "v_mfma_f32_32x32x16_bf16 v[0:15], v[80:83], v[116:119], v[0:15]\n"
    "ds_read_b128 v[112:115], v101 offset:32768\n"
    "ds_read_b64_tr_b16 v[64:65], v103 offset:36864\n"
    "s_waitcnt lgkmcnt(6)\n"
    "v_mfma_f32_32x32x16_bf16 v[16:31], v[84:87], v[116:119], v[16:31]\n"
    "ds_read_b64_tr_b16 v[66:67], v103 offset:37376\n"
    "ds_read_b64_tr_b16 v[68:69], v103 offset:37120\n"
    "s_waitcnt lgkmcnt(6)\n"
    "v_mfma_f32_32x32x16_bf16 v[32:47], v[88:91], v[116:119], v[32:47]\n"
    "ds_read_b64_tr_b16 v[70:71], v103 offset:37632\n"
    "ds_read_b64_tr_b16 v[72:73], v103 offset:45056\n"
    "s_waitcnt lgkmcnt(6)\n"
    "v_mfma_f32_32x32x16_bf16 v[48:63], v[92:95], v[116:119], v[48:63]\n"
    "ds_read_b64_tr_b16 v[74:75], v103 offset:45568\n"
    "ds_read_b64_tr_b16 v[76:77], v103 offset:45312\n"
    "ds_read_b64_tr_b16 v[78:79], v103 offset:45824\n"
    "s_waitcnt lgkmcnt(6)\n"
    "v_mfma_f32_32x32x16_bf16 v[0:15], v[64:67], v[112:115], v[0:15]\n"
    "ds_read_b128 v[116:119], v102 offset:32768\n"
    "ds_read_b64_tr_b16 v[80:81], v103 offset:38912\n"
    "s_waitcnt lgkmcnt(6)\n"
    "v_mfma_f32_32x32x16_bf16 v[16:31], v[68:71], v[112:115], v[16:31]\n"
    "ds_read_b64_tr_b16 v[82:83], v103 offset:39424\n"
    "ds_read_b64_tr_b16 v[84:85], v103 offset:39168\n"
    "s_waitcnt lgkmcnt(6)\n"
    "v_mfma_f32_32x32x16_bf16 v[32:47], v[72:75], v[112:115], v[32:47]\n"
    "ds_read_b64_tr_b16 v[86:87], v103 offset:39680\n"
    "ds_read_b64_tr_b16 v[88:89], v103 offset:47104\n"
    "s_waitcnt lgkmcnt(6)\n"
    "v_mfma_f32_32x32x16_bf16 v[48:63], v[76:79], v[112:115], v[48:63]\n"
    "ds_read_b64_tr_b16 v[90:91], v103 offset:47616\n"
    "ds_read_b64_tr_b16 v[92:93], v103 offset:47360\n"
    "ds_read_b64_tr_b16 v[94:95], v103 offset:47872\n"
    "s_waitcnt lgkmcnt(0)\n"
    "s_barrier\n"
    "v_mfma_f32_32x32x16_bf16 v[0:15], v[80:83], v[116:119], v[0:15]\n"
    "ds_read_b128 v[128:131], v99 offset:49152\n"
    "ds_read_b64_tr_b16 v[144:145], v103 offset:49152\n"
    "ds_read_b64_tr_b16 v[146:147], v103 offset:49664\n"
    "ds_read_b64_tr_b16 v[148:149], v103 offset:49408\n"
    "ds_read_b64_tr_b16 v[150:151], v103 offset:49920\n"
    "v_mfma_f32_32x32x16_bf16 v[16:31], v[84:87], v[116:119], v[16:31]\n"
    "ds_read_b128 v[132:135], v100 offset:49152\n"
    "ds_read_b64_tr_b16 v[160:161], v103 offset:51200\n"
    "ds_read_b64_tr_b16 v[162:163], v103 offset:51712\n"
    "ds_read_b64_tr_b16 v[164:165], v103 offset:51456\n"
    "ds_read_b64_tr_b16 v[166:167], v103 offset:51968\n"
    "v_mfma_f32_32x32x16_bf16 v[32:47], v[88:91], v[116:119], v[32:47]\n"
    "ds_read_b128 v[136:139], v101 offset:49152\n"
    "ds_read_b64_tr_b16 v[176:177], v103 offset:53248\n"
    "ds_read_b64_tr_b16 v[178:179], v103 offset:53760\n"
    "ds_read_b64_tr_b16 v[180:181], v103 offset:53504\n"
    "ds_read_b64_tr_b16 v[182:183], v103 offset:54016\n"
    "v_mfma_f32_32x32x16_bf16 v[48:63], v[92:95], v[116:119], v[48:63]\n"
    "ds_read_b128 v[140:143], v102 offset:49152\n"
    "ds_read_b64_tr_b16 v[192:193], v103 offset:55296\n"
    "ds_read_b64_tr_b16 v[194:195], v103 offset:55808\n"
    "ds_read_b64_tr_b16 v[196:197], v103 offset:55552\n"
    "ds_read_b64_tr_b16 v[198:199], v103 offset:56064\n"
    "10: s_cbranch_execz 10f\n"
    "s_waitcnt lgkmcnt(15)\n"
    "v_mfma_f32_32x32x16_bf16 v[0:15], v[144:147], v[128:131], v[0:15]\n"
    "ds_read_b64_tr_b16 v[152:153], v103 offset:57344\n"
    "ds_read_b64_tr_b16 v[154:155], v103 offset:57856\n"
    "v_mfma_f32_32x32x16_bf16 v[16:31], v[148:151], v[128:131], v[16:31]\n"
    "ds_read_b64_tr_b16 v[156:157], v103 offset:57600\n"
    "ds_read_b64_tr_b16 v[158:159], v103 offset:58112\n"
    "s_waitcnt lgkmcnt(14)\n"
    "v_mfma_f32_32x32x16_bf16 v[0:15], v[160:163], v[132:135], v[0:15]\n"
    "ds_read_b64_tr_b16 v[168:169], v103 offset:59392\n"
    "ds_read_b64_tr_b16 v[170:171], v103 offset:59904\n"
    "v_mfma_f32_32x32x16_bf16 v[16:31], v[164:167], v[132:135], v[16:31]\n"
    "ds_read_b64_tr_b16 v[172:173], v103 offset:59648\n"
    "ds_read_b64_tr_b16 v[174:175], v103 offset:60160\n"
    "s_waitcnt lgkmcnt(13)\n"
    "v_mfma_f32_32x32x16_bf16 v[0:15], v[176:179], v[136:139], v[0:15]\n"
    "ds_read_b64_tr_b16 v[184:185], v103 offset:61440\n"
    "ds_read_b64_tr_b16 v[186:187], v103 offset:61952\n"
    "v_mfma_f32_32x32x16_bf16 v[16:31], v[180:183], v[136:139], v[16:31]\n"
    "ds_read_b64_tr_b16 v[188:189], v103 offset:61696\n"
    "ds_read_b64_tr_b16 v[190:191], v103 offset:62208\n"
    "s_waitcnt lgkmcnt(12)\n"
    "v_mfma_f32_32x32x16_bf16 v[0:15], v[192:195], v[140:143], v[0:15]\n"
    "ds_read_b64_tr_b16 v[200:201], v103 offset:63488\n"
    "ds_read_b64_tr_b16 v[202:203], v103 offset:64000\n"
    "v_mfma_f32_32x32x16_bf16 v[16:31], v[196:199], v[140:143], v[16:31]\n"
    "ds_read_b64_tr_b16 v[204:205], v103 offset:63744\n"
    "ds_read_b64_tr_b16 v[206:207], v103 offset:64256\n"
    "11: s_cbranch_execz 11f\n"
    "s_waitcnt lgkmcnt(12)\n"
    "v_mfma_f32_32x32x16_bf16 v[32:47], v[152:155], v[128:131], v[32:47]\n"
    "v_readfirstlane_b32 s39, v104\n"
    "v_mfma_f32_32x32x16_bf16 v[48:63], v[156:159], v[128:131], v[48:63]\n"
    "s_waitcnt lgkmcnt(0)\n"
    "v_mfma_f32_32x32x16_bf16 v[32:47], v[168:171], v[132:135], v[32:47]\n"
    "s_nop 0\n"
    "v_pk_mul_f32 v[0:1], v[0:1], s[52:53]\n"
    "v_pk_mul_f32 v[2:3], v[2:3], s[52:53]\n"
    "v_pk_mul_f32 v[4:5], v[4:5], s[52:53]\n"
    "v_pk_mul_f32 v[6:7], v[6:7], s[52:53]\n"
    "v_cvt_pk_bf16_f32 v64, v0, v1\n"
    "v_cvt_pk_bf16_f32 v65, v2, v3\n"
    "v_cvt_pk_bf16_f32 v66, v4, v5\n"
    "v_mfma_f32_32x32x16_bf16 v[48:63], v[172:175], v[132:135], v[48:63]\n"
    "v_cvt_pk_bf16_f32 v67, v6, v7\n"
    "global_store_dwordx4 v208, v[64:67], s[48:49] offset:0 sc1\n"
    "s_add_u32 s48, s48, 131072\n"
    "s_addc_u32 s49, s49, 0\n"
    "v_pk_mul_f32 v[8:9], v[8:9], s[52:53]\n"
    "v_pk_mul_f32 v[10:11], v[10:11], s[52:53]\n"
    "v_pk_mul_f32 v[12:13], v[12:13], s[52:53]\n"
    "v_mfma_f32_32x32x16_bf16 v[32:47], v[184:187], v[136:139], v[32:47]\n"
    "v_pk_mul_f32 v[14:15], v[14:15], s[52:53]\n"
    "v_cvt_pk_bf16_f32 v68, v8, v9\n"
    "v_cvt_pk_bf16_f32 v69, v10, v11\n"
    "v_cvt_pk_bf16_f32 v70, v12, v13\n"
    "v_cvt_pk_bf16_f32 v71, v14, v15\n"
    "12: s_cbranch_execz 12f\n"
    "global_store_dwordx4 v208, v[68:71], s[48:49] offset:0 sc1\n"
    "s_add_u32 s48, s48, 131072\n"
    "v_mfma_f32_32x32x16_bf16 v[48:63], v[188:191], v[136:139], v[48:63]\n"
    "s_addc_u32 s49, s49, 0\n"
    "v_pk_mul_f32 v[16:17], v[16:17], s[52:53]\n"
    "v_pk_mul_f32 v[18:19], v[18:19], s[52:53]\n"
    "v_pk_mul_f32 v[20:21], v[20:21], s[52:53]\n"
    "v_pk_mul_f32 v[22:23], v[22:23], s[52:53]\n"
    "v_cvt_pk_bf16_f32 v72, v16, v17\n"
    "v_cvt_pk_bf16_f32 v73, v18, v19\n"
    "v_mfma_f32_32x32x16_bf16 v[32:47], v[200:203], v[140:143], v[32:47]\n"
    "v_cvt_pk_bf16_f32 v74, v20, v21\n"
    "v_cvt_pk_bf16_f32 v75, v22, v23\n"
    "global_store_dwordx4 v208, v[72:75], s[48:49] offset:0 sc1\n"
    "s_add_u32 s48, s48, 131072\n"
    "s_addc_u32 s49, s49, 0\n"
    "v_pk_mul_f32 v[24:25], v[24:25], s[52:53]\n"
    "v_pk_mul_f32 v[26:27], v[26:27], s[52:53]\n"
    "v_mfma_f32_32x32x16_bf16 v[48:63], v[204:207], v[140:143], v[48:63]\n"
    "v_pk_mul_f32 v[28:29], v[28:29], s[52:53]\n"
    "v_pk_mul_f32 v[30:31], v[30:31], s[52:53]\n"
    "v_cvt_pk_bf16_f32 v76, v24, v25\n"
    "v_cvt_pk_bf16_f32 v77, v26, v27\n"
    "v_cvt_pk_bf16_f32 v78, v28, v29\n"
    "v_cvt_pk_bf16_f32 v79, v30, v31\n"
    "global_store_dwordx4 v208, v[76:79], s[48:49] offset:0 sc1\n"
    "v_cvt_pk_bf16_f32 v80, v32, v33\n"
    "13: s_cbranch_execz 13f\n"
    "v_cvt_pk_bf16_f32 v81, v34, v35\n"
    "v_cvt_pk_bf16_f32 v82, v36, v37\n"
    "v_cvt_pk_bf16_f32 v83, v38, v39\n"
    "global_store_dwordx4 v105, v[80:83], s[50:51] offset:0 sc1\n"
    "s_add_u32 s50, s50, 65536\n"
    "s_addc_u32 s51, s51, 0\n"
    "v_cvt_pk_bf16_f32 v84, v40, v41\n"
    "v_cvt_pk_bf16_f32 v85, v42, v43\n"
    "v_cvt_pk_bf16_f32 v86, v44, v45\n"
    "v_cvt_pk_bf16_f32 v87, v46, v47\n"
    "global_store_dwordx4 v105, v[84:87], s[50:51] offset:0 sc1\n"
    "s_add_u32 s50, s50, 65536\n"
    "s_addc_u32 s51, s51, 0\n"
    "v_cvt_pk_bf16_f32 v88, v48, v49\n"
    "v_cvt_pk_bf16_f32 v89, v50, v51\n"
    "v_cvt_pk_bf16_f32 v90, v52, v53\n"
    "v_cvt_pk_bf16_f32 v91, v54, v55\n"
    "global_store_dwordx4 v105, v[88:91], s[50:51] offset:0 sc1\n"
    "s_add_u32 s50, s50, 65536\n"
    "s_addc_u32 s51, s51, 0\n"
    "v_cvt_pk_bf16_f32 v92, v56, v57\n"
    "v_cvt_pk_bf16_f32 v93, v58, v59\n"
    "v_cvt_pk_bf16_f32 v94, v60, v61\n"
    "v_cvt_pk_bf16_f32 v95, v62, v63\n"
    "global_store_dwordx4 v105, v[92:95], s[50:51] offset:0 sc1\n"
    "s_barrier\n"
    "s_add_u32 s82, s39, 1\n"
    "s_xor_b32 s83, s82, 1540483477\n"
    "s_branch A_\n"
    "A1_:\n"
    "v_add_u32 v103, 0x10000, v103\n"
    "v_mov_b32 v0, 0\n"
    "v_lshlrev_b32 v1, 2, v106\n"
    "v_and_b32 v1, 0x7c, v1\n"
    "v_add_u32 v1, 163712, v1\n"
    "ds_write_b32 v1, v0\n"
    "s_add_u32 s54, s42, 16\n"
    "s_addc_u32 s55, s43, 0\n"
    "s_mov_b64 exec, 0\n"
    "s_swappc_b64 s[44:45], s[54:55]\n"
    "s_mov_b64 exec, -1\n"
    "s_lshl_b32 s56, s32, 8\n"
    "s_add_u32 s56, s56, s38\n"
    "s_lshl_b32 s57, s56, 11\n"
    "s_add_u32 s57, s57, 2097152\n"
    "s_add_u32 s48, s24, s57\n"
    "s_addc_u32 s49, s25, 0\n"
    "v_bfe_u32 v128, v106, 4, 1\n"
    "v_lshlrev_b32 v128, 17, v128\n"
    "v_and_b32 v129, 0x2f0, v105\n"
    "v_add_u32 v128, v128, v129\n"
    "v_add_u32 v128, 0x400, v128\n"
    "s_waitcnt vmcnt(4) lgkmcnt(0)\n"
    "s_barrier\n"
    "ds_read_b128 v[112:115], v99 offset:0\n"
    "ds_read_b64_tr_b16 v[64:65], v103 offset:0\n"
    "ds_read_b64_tr_b16 v[66:67], v103 offset:512\n"
    "ds_read_b64_tr_b16 v[68:69], v103 offset:256\n"
    "ds_read_b64_tr_b16 v[70:71], v103 offset:768\n"
    "ds_read_b128 v[116:119], v100 offset:0\n"
    "ds_read_b64_tr_b16 v[72:73], v103 offset:2048\n"
    "ds_read_b64_tr_b16 v[74:75], v103 offset:2560\n"
    "ds_read_b64_tr_b16 v[76:77], v103 offset:2304\n"
    "ds_read_b64_tr_b16 v[78:79], v103 offset:2816\n"
    "ds_read_b128 v[120:123], v101 offset:0\n"
    "ds_read_b64_tr_b16 v[80:81], v103 offset:4096\n"
    "ds_read_b64_tr_b16 v[82:83], v103 offset:4608\n"
    "ds_read_b64_tr_b16 v[84:85], v103 offset:4352\n"
    "ds_read_b64_tr_b16 v[86:87], v103 offset:4864\n"
    "s_waitcnt lgkmcnt(12)\n"
    "v_mfma_f32_32x32x16_bf16 v[0:15], v[112:115], v[64:67], 0\n"
    "ds_read_b128 v[124:127], v102 offset:0\n"
    "ds_read_b64_tr_b16 v[88:89], v103 offset:6144\n"
    "s_waitcnt lgkmcnt(12)\n"
    "v_mfma_f32_32x32x16_bf16 v[16:31], v[112:115], v[68:71], 0\n"
    "ds_read_b64_tr_b16 v[90:91], v103 offset:6656\n"
    "ds_read_b64_tr_b16 v[92:93], v103 offset:6400\n"
    "ds_read_b64_tr_b16 v[94:95], v103 offset:6912\n"
    "s_waitcnt vmcnt(0) lgkmcnt(0)\n"
    "s_barrier\n"
    "v_mfma_f32_32x32x16_bf16 v[0:15], v[116:119], v[72:75], v[0:15]\n"
    "ds_read_b128 v[112:115], v99 offset:16384\n"
    "ds_read_b64_tr_b16 v[64:65], v103 offset:8192\n"
    "v_mfma_f32_32x32x16_bf16 v[16:31], v[116:119], v[76:79], v[16:31]\n"
    "ds_read_b64_tr_b16 v[66:67], v103 offset:8704\n"
    "ds_read_b64_tr_b16 v[68:69], v103 offset:8448\n"
    "ds_read_b64_tr_b16 v[70:71], v103 offset:8960\n"
    "v_mfma_f32_32x32x16_bf16 v[0:15], v[120:123], v[80:83], v[0:15]\n"
    "ds_read_b128 v[116:119], v100 offset:16384\n"
    "ds_read_b64_tr_b16 v[72:73], v103 offset:10240\n"
    "v_mfma_f32_32x32x16_bf16 v[16:31], v[120:123], v[84:87], v[16:31]\n"
    "ds_read_b64_tr_b16 v[74:75], v103 offset:10752\n"
    "ds_read_b64_tr_b16 v[76:77], v103 offset:10496\n"
    "ds_read_b64_tr_b16 v[78:79], v103 offset:11008\n"
    "v_mfma_f32_32x32x16_bf16 v[0:15], v[124:127], v[88:91], v[0:15]\n"
    "ds_read_b128 v[120:123], v101 offset:16384\n"
    "ds_read_b64_tr_b16 v[80:81], v103 offset:12288\n"
    "v_mfma_f32_32x32x16_bf16 v[16:31], v[124:127], v[92:95], v[16:31]\n"
    "ds_read_b64_tr_b16 v[82:83], v103 offset:12800\n"
    "ds_read_b64_tr_b16 v[84:85], v103 offset:12544\n"
    "ds_read_b64_tr_b16 v[86:87], v103 offset:13056\n"
    "s_waitcnt lgkmcnt(12)\n"
    "v_mfma_f32_32x32x16_bf16 v[0:15], v[112:115], v[64:67], v[0:15]\n"
    "ds_read_b128 v[124:127], v102 offset:16384\n"
    "ds_read_b64_tr_b16 v[88:89], v103 offset:14336\n"
    "s_waitcnt lgkmcnt(12)\n"
    "v_mfma_f32_32x32x16_bf16 v[16:31], v[112:115], v[68:71], v[16:31]\n"
    "ds_read_b64_tr_b16 v[90:91], v103 offset:14848\n"
    "ds_read_b64_tr_b16 v[92:93], v103 offset:14592\n"
    "ds_read_b64_tr_b16 v[94:95], v103 offset:15104\n"
    "s_waitcnt lgkmcnt(0)\n"
    "s_barrier\n"
    "v_mfma_f32_32x32x16_bf16 v[0:15], v[116:119], v[72:75], v[0:15]\n"
    "ds_read_b128 v[112:115], v99 offset:32768\n"
    "ds_read_b64_tr_b16 v[64:65], v103 offset:16384\n"
    "v_mfma_f32_32x32x16_bf16 v[16:31], v[116:119], v[76:79], v[16:31]\n"
    "ds_read_b64_tr_b16 v[66:67], v103 offset:16896\n"
    "ds_read_b64_tr_b16 v[68:69], v103 offset:16640\n"
    "ds_read_b64_tr_b16 v[70:71], v103 offset:17152\n"
    "v_mfma_f32_32x32x16_bf16 v[0:15], v[120:123], v[80:83], v[0:15]\n"
    "ds_read_b128 v[116:119], v100 offset:32768\n"
    "ds_read_b64_tr_b16 v[72:73], v103 offset:18432\n"
    "v_mfma_f32_32x32x16_bf16 v[16:31], v[120:123], v[84:87], v[16:31]\n"
    "ds_read_b64_tr_b16 v[74:75], v103 offset:18944\n"
    "ds_read_b64_tr_b16 v[76:77], v103 offset:18688\n"
    "ds_read_b64_tr_b16 v[78:79], v103 offset:19200\n"
    "v_mfma_f32_32x32x16_bf16 v[0:15], v[124:127], v[88:91], v[0:15]\n"
    "ds_read_b128 v[120:123], v101 offset:32768\n"
    "ds_read_b64_tr_b16 v[80:81], v103 offset:20480\n"
    "v_mfma_f32_32x32x16_bf16 v[16:31], v[124:127], v[92:95], v[16:31]\n"
    "ds_read_b64_tr_b16 v[82:83], v103 offset:20992\n"
    "ds_read_b64_tr_b16 v[84:85], v103 offset:20736\n"
    "ds_read_b64_tr_b16 v[86:87], v103 offset:21248\n"
    "s_waitcnt lgkmcnt(12)\n"
    "v_mfma_f32_32x32x16_bf16 v[0:15], v[112:115], v[64:67], v[0:15]\n"
    "ds_read_b128 v[124:127], v102 offset:32768\n"
    "ds_read_b64_tr_b16 v[88:89], v103 offset:22528\n"
    "s_waitcnt lgkmcnt(12)\n"
    "v_mfma_f32_32x32x16_bf16 v[16:31], v[112:115], v[68:71], v[16:31]\n"
    "ds_read_b64_tr_b16 v[90:91], v103 offset:23040\n"
    "ds_read_b64_tr_b16 v[92:93], v103 offset:22784\n"
    "ds_read_b64_tr_b16 v[94:95], v103 offset:23296\n"
    "s_waitcnt lgkmcnt(0)\n"
    "s_barrier\n"
    "v_mfma_f32_32x32x16_bf16 v[0:15], v[116:119], v[72:75], v[0:15]\n"
    "ds_read_b128 v[112:115], v99 offset:49152\n"
    "ds_read_b64_tr_b16 v[64:65], v103 offset:24576\n"
    "v_mfma_f32_32x32x16_bf16 v[16:31], v[116:119], v[76:79], v[16:31]\n"
    "ds_read_b64_tr_b16 v[66:67], v103 offset:25088\n"
    "ds_read_b64_tr_b16 v[68:69], v103 offset:24832\n"
    "ds_read_b64_tr_b16 v[70:71], v103 offset:25344\n"
    "v_mfma_f32_32x32x16_bf16 v[0:15], v[120:123], v[80:83], v[0:15]\n"
    "ds_read_b128 v[116:119], v100 offset:49152\n"
    "ds_read_b64_tr_b16 v[72:73], v103 offset:26624\n"
    "v_mfma_f32_32x32x16_bf16 v[16:31], v[120:123], v[84:87], v[16:31]\n"
    "ds_read_b64_tr_b16 v[74:75], v103 offset:27136\n"
    "ds_read_b64_tr_b16 v[76:77], v103 offset:26880\n"
    "ds_read_b64_tr_b16 v[78:79], v103 offset:27392\n"
    "v_mfma_f32_32x32x16_bf16 v[0:15], v[124:127], v[88:91], v[0:15]\n"
    "ds_read_b128 v[120:123], v101 offset:49152\n"
    "ds_read_b64_tr_b16 v[80:81], v103 offset:28672\n"
    "v_mfma_f32_32x32x16_bf16 v[16:31], v[124:127], v[92:95], v[16:31]\n"
    "ds_read_b64_tr_b16 v[82:83], v103 offset:29184\n"
    "ds_read_b64_tr_b16 v[84:85], v103 offset:28928\n"
    "ds_read_b64_tr_b16 v[86:87], v103 offset:29440\n"
    "s_waitcnt lgkmcnt(12)\n"
    "v_mfma_f32_32x32x16_bf16 v[0:15], v[112:115], v[64:67], v[0:15]\n"
    "ds_read_b128 v[124:127], v102 offset:49152\n"
    "ds_read_b64_tr_b16 v[88:89], v103 offset:30720\n"
    "s_waitcnt lgkmcnt(12)\n"
    "v_mfma_f32_32x32x16_bf16 v[16:31], v[112:115], v[68:71], v[16:31]\n"
    "ds_read_b64_tr_b16 v[90:91], v103 offset:31232\n"
    "ds_read_b64_tr_b16 v[92:93], v103 offset:30976\n"
    "ds_read_b64_tr_b16 v[94:95], v103 offset:31488\n"
    "s_waitcnt lgkmcnt(0)\n"
    "s_barrier\n"
    "v_mfma_f32_32x32x16_bf16 v[0:15], v[116:119], v[72:75], v[0:15]\n"
    "ds_read_b128 v[112:115], v99 offset:0\n"
    "ds_read_b64_tr_b16 v[64:65], v103 offset:0\n"
    "v_mfma_f32_32x32x16_bf16 v[16:31], v[116:119], v[76:79], v[16:31]\n"
    "ds_read_b64_tr_b16 v[66:67], v103 offset:512\n"
    "ds_read_b64_tr_b16 v[68:69], v103 offset:256\n"
    "ds_read_b64_tr_b16 v[70:71], v103 offset:768\n"
    "v_mfma_f32_32x32x16_bf16 v[0:15], v[120:123], v[80:83], v[0:15]\n"
    "ds_read_b128 v[116:119], v100 offset:0\n"
    "ds_read_b64_tr_b16 v[72:73], v103 offset:2048\n"
    "v_mfma_f32_32x32x16_bf16 v[16:31], v[120:123], v[84:87], v[16:31]\n"
    "ds_read_b64_tr_b16 v[74:75], v103 offset:2560\n"
    "ds_read_b64_tr_b16 v[76:77], v103 offset:2304\n"
    "ds_read_b64_tr_b16 v[78:79], v103 offset:2816\n"
    "v_mfma_f32_32x32x16_bf16 v[0:15], v[124:127], v[88:91], v[0:15]\n"
    "ds_read_b128 v[120:123], v101 offset:0\n"
    "ds_read_b64_tr_b16 v[80:81], v103 offset:4096\n"
    "v_mfma_f32_32x32x16_bf16 v[16:31], v[124:127], v[92:95], v[16:31]\n"
    "ds_read_b64_tr_b16 v[82:83], v103 offset:4608\n"
    "ds_read_b64_tr_b16 v[84:85], v103 offset:4352\n"
    "ds_read_b64_tr_b16 v[86:87], v103 offset:4864\n"
    "s_waitcnt lgkmcnt(12)\n"
    "v_mfma_f32_32x32x16_bf16 v[0:15], v[112:115], v[64:67], v[0:15]\n"
    "ds_read_b128 v[124:127], v102 offset:0\n"
    "ds_read_b64_tr_b16 v[88:89], v103 offset:6144\n"
    "s_waitcnt lgkmcnt(12)\n"
    "v_mfma_f32_32x32x16_bf16 v[16:31], v[112:115], v[68:71], v[16:31]\n"
    "ds_read_b64_tr_b16 v[90:91], v103 offset:6656\n"
    "ds_read_b64_tr_b16 v[92:93], v103 offset:6400\n"
    "ds_read_b64_tr_b16 v[94:95], v103 offset:6912\n"
    "s_waitcnt lgkmcnt(0)\n"
    "s_barrier\n"
    "v_mfma_f32_32x32x16_bf16 v[0:15], v[116:119], v[72:75], v[0:15]\n"
    "ds_read_b128 v[112:115], v99 offset:16384\n"
    "ds_read_b64_tr_b16 v[64:65], v103 offset:8192\n"
    "v_mfma_f32_32x32x16_bf16 v[16:31], v[116:119], v[76:79], v[16:31]\n"
    "ds_read_b64_tr_b16 v[66:67], v103 offset:8704\n"
    "ds_read_b64_tr_b16 v[68:69], v103 offset:8448\n"
    "ds_read_b64_tr_b16 v[70:71], v103 offset:8960\n"
    "v_mfma_f32_32x32x16_bf16 v[0:15], v[120:123], v[80:83], v[0:15]\n"
    "ds_read_b128 v[116:119], v100 offset:16384\n"
    "ds_read_b64_tr_b16 v[72:73], v103 offset:10240\n"
    "v_mfma_f32_32x32x16_bf16 v[16:31], v[120:123], v[84:87], v[16:31]\n"
    "ds_read_b64_tr_b16 v[74:75], v103 offset:10752\n"
    "ds_read_b64_tr_b16 v[76:77], v103 offset:10496\n"
    "ds_read_b64_tr_b16 v[78:79], v103 offset:11008\n"
    "v_mfma_f32_32x32x16_bf16 v[0:15], v[124:127], v[88:91], v[0:15]\n"
    "ds_read_b128 v[120:123], v101 offset:16384\n"
    "ds_read_b64_tr_b16 v[80:81], v103 offset:12288\n"
    "v_mfma_f32_32x32x16_bf16 v[16:31], v[124:127], v[92:95], v[16:31]\n"
    "ds_read_b64_tr_b16 v[82:83], v103 offset:12800\n"
    "ds_read_b64_tr_b16 v[84:85], v103 offset:12544\n"
    "ds_read_b64_tr_b16 v[86:87], v103 offset:13056\n"
    "s_waitcnt lgkmcnt(12)\n"
    "v_mfma_f32_32x32x16_bf16 v[0:15], v[112:115], v[64:67], v[0:15]\n"
    "ds_read_b128 v[124:127], v102 offset:16384\n"
    "ds_read_b64_tr_b16 v[88:89], v103 offset:14336\n"
    "s_waitcnt lgkmcnt(12)\n"
    "v_mfma_f32_32x32x16_bf16 v[16:31], v[112:115], v[68:71], v[16:31]\n"
    "ds_read_b64_tr_b16 v[90:91], v103 offset:14848\n"
    "ds_read_b64_tr_b16 v[92:93], v103 offset:14592\n"
    "ds_read_b64_tr_b16 v[94:95], v103 offset:15104\n"
    "s_waitcnt lgkmcnt(0)\n"
    "s_barrier\n"
    "v_mfma_f32_32x32x16_bf16 v[0:15], v[116:119], v[72:75], v[0:15]\n"
    "ds_read_b128 v[112:115], v99 offset:32768\n"
    "ds_read_b64_tr_b16 v[64:65], v103 offset:16384\n"
    "v_mfma_f32_32x32x16_bf16 v[16:31], v[116:119], v[76:79], v[16:31]\n"
    "ds_read_b64_tr_b16 v[66:67], v103 offset:16896\n"
    "ds_read_b64_tr_b16 v[68:69], v103 offset:16640\n"
    "ds_read_b64_tr_b16 v[70:71], v103 offset:17152\n"
    "v_mfma_f32_32x32x16_bf16 v[0:15], v[120:123], v[80:83], v[0:15]\n"
    "ds_read_b128 v[116:119], v100 offset:32768\n"
    "ds_read_b64_tr_b16 v[72:73], v103 offset:18432\n"
    "v_mfma_f32_32x32x16_bf16 v[16:31], v[120:123], v[84:87], v[16:31]\n"
    "ds_read_b64_tr_b16 v[74:75], v103 offset:18944\n"
    "ds_read_b64_tr_b16 v[76:77], v103 offset:18688\n"
    "ds_read_b64_tr_b16 v[78:79], v103 offset:19200\n"
    "v_mfma_f32_32x32x16_bf16 v[0:15], v[124:127], v[88:91], v[0:15]\n"
    "ds_read_b128 v[120:123], v101 offset:32768\n"
    "ds_read_b64_tr_b16 v[80:81], v103 offset:20480\n"
    "v_mfma_f32_32x32x16_bf16 v[16:31], v[124:127], v[92:95], v[16:31]\n"
    "ds_read_b64_tr_b16 v[82:83], v103 offset:20992\n"
    "ds_read_b64_tr_b16 v[84:85], v103 offset:20736\n"
    "ds_read_b64_tr_b16 v[86:87], v103 offset:21248\n"
    "s_waitcnt lgkmcnt(12)\n"
    "v_mfma_f32_32x32x16_bf16 v[0:15], v[112:115], v[64:67], v[0:15]\n"
    "ds_read_b128 v[124:127], v102 offset:32768\n"
    "ds_read_b64_tr_b16 v[88:89], v103 offset:22528\n"
    "s_waitcnt lgkmcnt(12)\n"
    "v_mfma_f32_32x32x16_bf16 v[16:31], v[112:115], v[68:71], v[16:31]\n"
    "ds_read_b64_tr_b16 v[90:91], v103 offset:23040\n"
    "ds_read_b64_tr_b16 v[92:93], v103 offset:22784\n"
    "ds_read_b64_tr_b16 v[94:95], v103 offset:23296\n"
    "s_waitcnt lgkmcnt(0)\n"
    "s_barrier\n"
    "v_mfma_f32_32x32x16_bf16 v[0:15], v[116:119], v[72:75], v[0:15]\n"
    "ds_read_b128 v[112:115], v99 offset:49152\n"
    "ds_read_b64_tr_b16 v[64:65], v103 offset:24576\n"
    "v_mfma_f32_32x32x16_bf16 v[16:31], v[116:119], v[76:79], v[16:31]\n"
    "ds_read_b64_tr_b16 v[66:67], v103 offset:25088\n"
    "ds_read_b64_tr_b16 v[68:69], v103 offset:24832\n"
    "ds_read_b64_tr_b16 v[70:71], v103 offset:25344\n"
    "v_mfma_f32_32x32x16_bf16 v[0:15], v[120:123], v[80:83], v[0:15]\n"
    "ds_read_b128 v[116:119], v100 offset:49152\n"
    "ds_read_b64_tr_b16 v[72:73], v103 offset:26624\n"
    "v_mfma_f32_32x32x16_bf16 v[16:31], v[120:123], v[84:87], v[16:31]\n"
    "ds_read_b64_tr_b16 v[74:75], v103 offset:27136\n"
    "ds_read_b64_tr_b16 v[76:77], v103 offset:26880\n"
    "ds_read_b64_tr_b16 v[78:79], v103 offset:27392\n"
    "v_mfma_f32_32x32x16_bf16 v[0:15], v[124:127], v[88:91], v[0:15]\n"
    "ds_read_b128 v[120:123], v101 offset:49152\n"
    "ds_read_b64_tr_b16 v[80:81], v103 offset:28672\n"
    "v_mfma_f32_32x32x16_bf16 v[16:31], v[124:127], v[92:95], v[16:31]\n"
    "ds_read_b64_tr_b16 v[82:83], v103 offset:29184\n"
    "ds_read_b64_tr_b16 v[84:85], v103 offset:28928\n"
    "ds_read_b64_tr_b16 v[86:87], v103 offset:29440\n"
    "s_waitcnt lgkmcnt(12)\n"
    "v_mfma_f32_32x32x16_bf16 v[0:15], v[112:115], v[64:67], v[0:15]\n"
    "ds_read_b128 v[124:127], v102 offset:49152\n"
    "ds_read_b64_tr_b16 v[88:89], v103 offset:30720\n"
    "s_waitcnt lgkmcnt(12)\n"
    "v_mfma_f32_32x32x16_bf16 v[16:31], v[112:115], v[68:71], v[16:31]\n"
    "ds_read_b64_tr_b16 v[90:91], v103 offset:31232\n"
    "ds_read_b64_tr_b16 v[92:93], v103 offset:30976\n"
    "ds_read_b64_tr_b16 v[94:95], v103 offset:31488\n"
    "s_barrier\n"
    "s_waitcnt lgkmcnt(12)\n"
    "v_mfma_f32_32x32x16_bf16 v[0:15], v[116:119], v[72:75], v[0:15]\n"
    "s_waitcnt lgkmcnt(10)\n"
    "v_mfma_f32_32x32x16_bf16 v[16:31], v[116:119], v[76:79], v[16:31]\n"
    "s_waitcnt lgkmcnt(7)\n"
    "v_mfma_f32_32x32x16_bf16 v[0:15], v[120:123], v[80:83], v[0:15]\n"
    "s_waitcnt lgkmcnt(5)\n"
    "v_mfma_f32_32x32x16_bf16 v[16:31], v[120:123], v[84:87], v[16:31]\n"
    "s_waitcnt lgkmcnt(2)\n"
    "v_mfma_f32_32x32x16_bf16 v[0:15], v[124:127], v[88:91], v[0:15]\n"
    "s_waitcnt lgkmcnt(0)\n"
    "v_mfma_f32_32x32x16_bf16 v[16:31], v[124:127], v[92:95], v[16:31]\n"
    "14: s_cbranch_execz 14f\n"
    "v_readfirstlane_b32 s39, v104\n"
    "s_nop 8\n"
    "v_cvt_pk_bf16_f32 v64, v0, v1\n"
    "v_cvt_pk_bf16_f32 v65, v2, v3\n"
    "v_cvt_pk_bf16_f32 v66, v4, v5\n"
    "v_cvt_pk_bf16_f32 v67, v6, v7\n"
    "global_store_dwordx4 v128, v[64:67], s[48:49] offset:0 sc1\n"
    "v_cvt_pk_bf16_f32 v68, v8, v9\n"
    "v_cvt_pk_bf16_f32 v69, v10, v11\n"
    "v_cvt_pk_bf16_f32 v70, v12, v13\n"
    "v_cvt_pk_bf16_f32 v71, v14, v15\n"
    "global_store_dwordx4 v128, v[68:71], s[48:49] offset:256 sc1\n"
    "s_add_u32 s48, s48, 262144\n"
    "s_addc_u32 s49, s49, 0\n"
    "v_cvt_pk_bf16_f32 v72, v16, v17\n"
    "v_cvt_pk_bf16_f32 v73, v18, v19\n"
    "v_cvt_pk_bf16_f32 v74, v20, v21\n"
    "v_cvt_pk_bf16_f32 v75, v22, v23\n"
    "global_store_dwordx4 v128, v[72:75], s[48:49] offset:0 sc1\n"
    "v_cvt_pk_bf16_f32 v76, v24, v25\n"
    "v_cvt_pk_bf16_f32 v77, v26, v27\n"
    "v_cvt_pk_bf16_f32 v78, v28, v29\n"
    "v_cvt_pk_bf16_f32 v79, v30, v31\n"
    "global_store_dwordx4 v128, v[76:79], s[48:49] offset:256 sc1\n"
    "s_add_u32 s82, s39, 1\n"
    "s_xor_b32 s83, s82, 1540483477\n"
    "A_:\n"
    "s_mul_i32 s56, s33, 16\n"
    "s_add_u32 s56, s56, 32\n"
    "s_add_u32 s54, s42, s56\n"
    "s_addc_u32 s55, s43, 0\n"
    "s_mov_b64 exec, 0\n"
    "s_swappc_b64 s[44:45], s[54:55]\n"
    "s_mov_b64 exec, -1\n"
    "s_lshl1_add_u32 s56, s32, s33\n"
    "s_lshl4_add_u32 s56, s56, s34\n"
    "s_lshl_b32 s56, s56, 7\n"
    "s_add_u32 s56, s56, 17825792\n"
    "s_add_u32 s52, s24, s56\n"
    "s_addc_u32 s53, s25, 0\n"
    "s_waitcnt vmcnt(0)\n"
    "s_mov_b64 exec, 1\n"
    "ds_add_rtn_u32 v112, v108, v109 offset:0\n"
    "v_mov_b64 v[240:241], s[82:83]\n"
    "v_mov_b32 v113, 0\n"
    "s_waitcnt lgkmcnt(0)\n"
    "v_cmp_eq_u32 vcc, 3, v112\n"
    "s_cbranch_vccz U18\n"
    "global_store_dwordx2 v113, v[240:241], s[52:53] sc1\n"
    "U18:\n"
    "s_mov_b64 exec, -1\n"
    "s_cmp_eq_u32 s33, 0\n"
    "s_cbranch_scc1 J15\n"
    "s_barrier\n"
    "J15:\n"
    "31: s_cbranch_execz 31f\n"
    "v_mov_b64 v[0:1], 0\n"
    "v_mov_b64 v[2:3], 0\n"
    "v_mov_b64 v[4:5], 0\n"
    "v_mov_b64 v[6:7], 0\n"
    "v_mov_b64 v[8:9], 0\n"
    "v_mov_b64 v[10:11], 0\n"
    "v_mov_b64 v[12:13], 0\n"
    "v_mov_b64 v[14:15], 0\n"
    "v_mov_b64 v[16:17], 0\n"
    "v_mov_b64 v[18:19], 0\n"
    "v_mov_b64 v[20:21], 0\n"
    "v_mov_b64 v[22:23], 0\n"
    "v_mov_b64 v[24:25], 0\n"
    "v_mov_b64 v[26:27], 0\n"
    "v_mov_b64 v[28:29], 0\n"
    "v_mov_b64 v[30:31], 0\n"
    "v_mov_b64 v[88:89], 0\n"
    "v_mov_b64 v[90:91], 0\n"
    "v_mov_b64 v[92:93], 0\n"
    "v_mov_b64 v[94:95], 0\n"
    "v_and_b32 v192, 27, v106\n"
    "v_cmp_eq_u32 vcc, 16, v192\n"
    "v_mov_b32 v193, 0x3f803f80\n"
    "v_mov_b32 v250, 0xdddddddd\n"
    "v_cndmask_b32 v196, 0, v193, vcc\n"
    "v_mov_b32 v197, v196\n"
    "v_mov_b64 v[198:199], v[196:197]\n"
    "32: s_cbranch_execz 32f\n"
    "v_mov_b64 v[200:201], v[196:197]\n"
    "v_mov_b64 v[202:203], v[196:197]\n"
    "v_mov_b64 v[204:205], v[196:197]\n"
    "v_mov_b64 v[206:207], v[196:197]\n"
    "s_lshl_b32 s56, s29, 16\n"
    "v_add_u32 v104, s56, v105\n"
    "s_lshl_b32 s56, s29, 16\n"
    "s_lshl_b32 s57, s28, 10\n"
    "s_add_u32 s58, s56, s57\n"
    "s_lshl_b32 s56, s36, 17\n"
    "s_add_u32 s56, s56, s58\n"
    "s_add_u32 s56, s56, 2097152\n"
    "s_add_u32 s50, s24, s56\n"
    "s_addc_u32 s51, s25, 0\n"
    "s_lshl2_add_u32 s56, s37, s28\n"
    "s_lshl1_add_u32 s70, s56, s29\n"
    "s_lshl_b32 s56, s56, 11\n"
    "s_lshl_b32 s57, s36, 16\n"
    "s_add_u32 s56, s56, s57\n"
    "s_add_u32 s48, s24, s56\n"
    "s_addc_u32 s49, s25, 0\n"
    "s_cmp_lg_u32 s27, 0\n"
    "s_cbranch_scc1 J16\n"
    "s_lshl_b32 s56, s30, 12\n"
    "s_add_u32 s56, s56, 17825792\n"
    "s_add_u32 s52, s24, s56\n"
    "s_addc_u32 s53, s25, 0\n"
    "v_and_b32 v115, 31, v106\n"
    "v_lshlrev_b32 v115, 7, v115\n"
    "s_mov_b32 s60, 0x100000\n"
    "P19:\n"
    "global_load_dwordx2 v[112:113], v115, s[52:53] sc1\n"
    "s_sub_u32 s60, s60, 1\n"
    "s_cbranch_scc1 P19x\n"
    "s_waitcnt vmcnt(0)\n"
    "v_cmp_ne_u64 vcc, s[82:83], v[112:113]\n"
    "s_cbranch_vccnz P19\n"
    "P19x:\n"
    "s_mov_b64 exec, 1\n"
    "ds_add_u32 v108, v109 offset:8\n"
    "s_mov_b64 exec, -1\n"
    "33: s_cbranch_execz 33f\n"
    "s_branch J17\n"
    "J16:\n"
    "s_add_u32 s54, s42, 64\n"
    "s_addc_u32 s55, s43, 0\n"
    "s_mov_b64 exec, 0\n"
    "s_swappc_b64 s[44:45], s[54:55]\n"
    "s_mov_b64 exec, -1\n"
    "s_mov_b32 s60, 0x100000\n"
    "W20:\n"
    "ds_read_b32 v112, v108 offset:8\n"
    "s_sub_u32 s60, s60, 1\n"
    "s_cbranch_scc1 W20x\n"
    "s_waitcnt lgkmcnt(0)\n"
    "v_readfirstlane_b32 s59, v112\n"
    "s_cmp_lt_u32 s59, 1\n"
    "s_cbranch_scc1 W20\n"
    "W20x:\n"
    "J17:\n"
    "s_cmp_lg_u32 s33, 0\n"
    "s_cbranch_scc1 J21\n"
    "s_mov_b32 s60, 0x100000\n"
    "W22:\n"
    "ds_read_b32 v112, v108 offset:16\n"
    "s_sub_u32 s60, s60, 1\n"
    "s_cbranch_scc1 W22x\n"
    "s_waitcnt lgkmcnt(0)\n"
    "v_readfirstlane_b32 s59, v112\n"
    "s_cmp_lt_u32 s59, 4\n"
    "s_cbranch_scc1 W22\n"
    "W22x:\n"
    "J21:\n"
    "s_mov_b32 m0, s58\n"
    "s_mov_b32 s41, 5\n"
    "DL_23:\n"
    "global_load_lds_dwordx4 v105, s[50:51] sc1\n"
    "s_add_u32 m0, m0, 0x1000\n"
    "s_add_u32 s50, s50, 4096\n"
    "s_addc_u32 s51, s51, 0\n"
    "s_sub_u32 s41, s41, 1\n"
    "s_cmp_lg_u32 s41, 0\n"
    "s_cbranch_scc1 DL_23\n"
    "global_load_dwordx4 v[96:99], v105, s[48:49] sc1\n"
    "global_load_dwordx4 v[100:103], v105, s[48:49] offset:1024 sc1\n"
    "s_setprio 2\n"
    "s_mov_b32 s60, 0x100000\n"
    "W24:\n"
    "ds_read_b32 v112, v108 offset:16\n"
    "s_sub_u32 s60, s60, 1\n"
    "s_cbranch_scc1 W24x\n"
    "s_waitcnt lgkmcnt(0)\n"
    "v_readfirstlane_b32 s59, v112\n"
    "s_cmp_lt_u32 s59, 4\n"
    "s_cbranch_scc1 W24\n"
    "W24x:\n"
    "31: s_cbranch_execz 31f\n"
    "s_add_u32 s58, s58, 20480\n"
    "s_mov_b32 m0, s58\n"
    "s_mov_b32 s41, 11\n"
    "DL_25:\n"
    "global_load_lds_dwordx4 v105, s[50:51] sc1\n"
    "s_add_u32 m0, m0, 0x1000\n"
    "s_add_u32 s50, s50, 4096\n"
    "s_addc_u32 s51, s51, 0\n"
    "s_sub_u32 s41, s41, 1\n"
    "s_cmp_lg_u32 s41, 0\n"
    "s_cbranch_scc1 DL_25\n"
    "s_waitcnt vmcnt(11)\n"
    "s_mov_b64 exec, 1\n"
    "ds_add_u32 v108, v109 offset:24\n"
    "s_mov_b64 exec, -1\n"
    "s_mov_b32 s60, 0x100000\n"
    "W26:\n"
    "ds_read_b32 v112, v108 offset:24\n"
    "s_sub_u32 s60, s60, 1\n"
    "s_cbranch_scc1 W26x\n"
    "s_waitcnt lgkmcnt(0)\n"
    "v_readfirstlane_b32 s59, v112\n"
    "s_cmp_lt_u32 s59, 4\n"
    "s_cbranch_scc1 W26\n"
    "W26x:\n"
    "AT_:\n"
    "ds_read_b128 v[192:195], v104\n"
    "s_mov_b64 exec, s[46:47]\n"
    "ds_read_b128 v[196:199], v104 offset:1024\n"
    "s_mov_b64 exec, -1\n"
    "s_mov_b32 s40, 0\n"
    "s_cmp_eq_u32 s29, 0\n"
    "s_cselect_b32 s63, 38, 130\n"
    "s_and_b32 s56, s33, s29\n"
    "s_lshl_b32 s56, s56, 1\n"
    "s_andn2_b32 s63, s63, s56\n"
    "s_or_b32 s63, s63, 0x150\n"
    "s_waitcnt lgkmcnt(0)\n"
    "v_mfma_f32_32x32x16_bf16 v[32:47], v[192:195], v[96:99], 0\n"
    "32: s_cbranch_execz 32f\n"
    "s_nop 8\n"
    ".p2align 5\n"
    "SG_:\n"
    "s_nop 2\n"
    "v_exp_f32 v32, v32\n"
    "v_exp_f32 v33, v33\n"
    "v_exp_f32 v34, v34\n"
    "v_exp_f32 v35, v35\n"
    "v_exp_f32 v36, v36\n"
    "v_mfma_f32_32x32x16_bf16 v[48:63], v[192:195], v[100:103], 0\n"
    "ds_read_b128 v[192:195], v104 offset:2048\n"
    "v_exp_f32 v37, v37\n"
    "v_exp_f32 v38, v38\n"
    "v_exp_f32 v39, v39\n"
    "v_exp_f32 v40, v40\n"
    "v_exp_f32 v41, v41\n"
    "v_exp_f32 v42, v42\n"
    "v_smfmac_f32_32x32x32_bf16 v[16:31], v[204:207], v[88:95], v250\n"
    "s_mov_b64 exec, s[46:47]\n"
    "ds_read_b128 v[204:207], v104 offset:1280\n"
    "ds_read_b128 v[200:203], v104 offset:3072\n"
    "s_mov_b64 exec, -1\n"
    "v_exp_f32 v43, v43\n"
    "v_exp_f32 v44, v44\n"
    "v_exp_f32 v45, v45\n"
    "v_exp_f32 v46, v46\n"
    "v_exp_f32 v47, v47\n"
    "v_smfmac_f32_32x32x32_bf16 v[0:15], v[196:199], v[32:39], v250\n"
    "s_waitcnt lgkmcnt(2)\n"
    "v_exp_f32 v48, v48\n"
    "v_exp_f32 v49, v49\n"
    "v_exp_f32 v50, v50\n"
    "v_exp_f32 v51, v51\n"
    "v_exp_f32 v52, v52\n"
    "v_mfma_f32_32x32x16_bf16 v[64:79], v[192:195], v[96:99], 0\n"
    "s_waitcnt lgkmcnt(0)\n"
    "v_exp_f32 v53, v53\n"
    "v_exp_f32 v54, v54\n"
    "v_exp_f32 v55, v55\n"
    "v_exp_f32 v56, v56\n"
    "v_exp_f32 v57, v57\n"
    "v_exp_f32 v58, v58\n"
    "v_smfmac_f32_32x32x32_bf16 v[0:15], v[204:207], v[40:47], v250\n"
    "v_exp_f32 v59, v59\n"
    "v_exp_f32 v60, v60\n"
    "v_exp_f32 v61, v61\n"
    "v_exp_f32 v62, v62\n"
    "v_exp_f32 v63, v63\n"
    "v_smfmac_f32_32x32x32_bf16 v[16:31], v[196:199], v[48:55], v250\n"
    "33: s_cbranch_execz 33f\n"
    "v_exp_f32 v64, v64\n"
    "v_exp_f32 v65, v65\n"
    "v_exp_f32 v66, v66\n"
    "v_exp_f32 v67, v67\n"
    "v_exp_f32 v68, v68\n"
    "v_mfma_f32_32x32x16_bf16 v[80:95], v[192:195], v[100:103], 0\n"
    "ds_read_b128 v[192:195], v104 offset:4096\n"
    "v_exp_f32 v69, v69\n"
    "v_exp_f32 v70, v70\n"
    "v_exp_f32 v71, v71\n"
    "v_exp_f32 v72, v72\n"
    "v_exp_f32 v73, v73\n"
    "v_exp_f32 v74, v74\n"
    "v_smfmac_f32_32x32x32_bf16 v[16:31], v[204:207], v[56:63], v250\n"
    "s_mov_b64 exec, s[46:47]\n"
    "ds_read_b128 v[204:207], v104 offset:3328\n"
    "ds_read_b128 v[196:199], v104 offset:5120\n"
    "s_mov_b64 exec, -1\n"
    "v_exp_f32 v75, v75\n"
    "v_exp_f32 v76, v76\n"
    "v_exp_f32 v77, v77\n"
    "v_exp_f32 v78, v78\n"
    "v_exp_f32 v79, v79\n"
    "v_smfmac_f32_32x32x32_bf16 v[0:15], v[200:203], v[64:71], v250\n"
    "s_waitcnt lgkmcnt(2)\n"
    "v_exp_f32 v80, v80\n"
    "v_exp_f32 v81, v81\n"
    "v_exp_f32 v82, v82\n"
    "v_exp_f32 v83, v83\n"
    "v_exp_f32 v84, v84\n"
    "v_mfma_f32_32x32x16_bf16 v[32:47], v[192:195], v[96:99], 0\n"
    "s_waitcnt lgkmcnt(0)\n"
    "v_exp_f32 v85, v85\n"
    "v_exp_f32 v86, v86\n"
    "v_exp_f32 v87, v87\n"
    "v_exp_f32 v88, v88\n"
    "v_exp_f32 v89, v89\n"
    "v_exp_f32 v90, v90\n"
    "v_smfmac_f32_32x32x32_bf16 v[0:15], v[204:207], v[72:79], v250\n"
    "v_exp_f32 v91, v91\n"
    "v_exp_f32 v92, v92\n"
    "v_exp_f32 v93, v93\n"
    "v_exp_f32 v94, v94\n"
    "v_exp_f32 v95, v95\n"
    "v_smfmac_f32_32x32x32_bf16 v[16:31], v[200:203], v[80:87], v250\n"
    "31: s_cbranch_execz 31f\n"
    "v_exp_f32 v32, v32\n"
    "v_exp_f32 v33, v33\n"
    "v_exp_f32 v34, v34\n"
    "v_exp_f32 v35, v35\n"
    "v_exp_f32 v36, v36\n"
    "v_mfma_f32_32x32x16_bf16 v[48:63], v[192:195], v[100:103], 0\n"
    "ds_read_b128 v[192:195], v104 offset:6144\n"
    "v_exp_f32 v37, v37\n"
    "v_exp_f32 v38, v38\n"
    "v_exp_f32 v39, v39\n"
    "v_exp_f32 v40, v40\n"
    "v_exp_f32 v41, v41\n"
    "v_exp_f32 v42, v42\n"
    "v_smfmac_f32_32x32x32_bf16 v[16:31], v[204:207], v[88:95], v250\n"
    "s_mov_b64 exec, s[46:47]\n"
    "ds_read_b128 v[204:207], v104 offset:5376\n"
    "ds_read_b128 v[200:203], v104 offset:7168\n"
    "s_mov_b64 exec, -1\n"
    "v_exp_f32 v43, v43\n"
    "v_exp_f32 v44, v44\n"
    "v_exp_f32 v45, v45\n"
    "v_exp_f32 v46, v46\n"
    "v_exp_f32 v47, v47\n"
    "v_smfmac_f32_32x32x32_bf16 v[0:15], v[196:199], v[32:39], v250\n"
    "s_waitcnt lgkmcnt(2)\n"
    "v_exp_f32 v48, v48\n"
    "v_exp_f32 v49, v49\n"
    "v_exp_f32 v50, v50\n"
    "v_exp_f32 v51, v51\n"
    "v_exp_f32 v52, v52\n"
    "v_mfma_f32_32x32x16_bf16 v[64:79], v[192:195], v[96:99], 0\n"
    "s_waitcnt lgkmcnt(0)\n"
    "v_exp_f32 v53, v53\n"
    "v_exp_f32 v54, v54\n"
    "v_exp_f32 v55, v55\n"
    "v_exp_f32 v56, v56\n"
    "v_exp_f32 v57, v57\n"
    "v_exp_f32 v58, v58\n"
    "v_smfmac_f32_32x32x32_bf16 v[0:15], v[204:207], v[40:47], v250\n"
    "s_add_u32 s40, s40, 1\n"
    "v_exp_f32 v59, v59\n"
    "v_exp_f32 v60, v60\n"
    "v_exp_f32 v61, v61\n"
    "v_exp_f32 v62, v62\n"
    "v_exp_f32 v63, v63\n"
    "v_smfmac_f32_32x32x32_bf16 v[16:31], v[196:199], v[48:55], v250\n"
    "32: s_cbranch_execz 32f\n"
    "v_exp_f32 v64, v64\n"
    "v_exp_f32 v65, v65\n"
    "v_exp_f32 v66, v66\n"
    "v_exp_f32 v67, v67\n"
    "v_exp_f32 v68, v68\n"
    "v_mfma_f32_32x32x16_bf16 v[80:95], v[192:195], v[100:103], 0\n"
    "ds_read_b128 v[192:195], v104 offset:8192\n"
    "v_exp_f32 v69, v69\n"
    "v_exp_f32 v70, v70\n"
    "v_exp_f32 v71, v71\n"
    "v_exp_f32 v72, v72\n"
    "v_exp_f32 v73, v73\n"
    "v_exp_f32 v74, v74\n"
    "v_smfmac_f32_32x32x32_bf16 v[16:31], v[204:207], v[56:63], v250\n"
    "s_mov_b64 exec, s[46:47]\n"
    "ds_read_b128 v[204:207], v104 offset:7424\n"
    "ds_read_b128 v[196:199], v104 offset:9216\n"
    "s_mov_b64 exec, -1\n"
    "v_exp_f32 v75, v75\n"
    "v_exp_f32 v76, v76\n"
    "v_exp_f32 v77, v77\n"
    "v_exp_f32 v78, v78\n"
    "v_exp_f32 v79, v79\n"
    "v_smfmac_f32_32x32x32_bf16 v[0:15], v[200:203], v[64:71], v250\n"
    "s_waitcnt lgkmcnt(2)\n"
    "v_exp_f32 v80, v80\n"
    "v_exp_f32 v81, v81\n"
    "v_exp_f32 v82, v82\n"
    "v_exp_f32 v83, v83\n"
    "v_exp_f32 v84, v84\n"
    "v_mfma_f32_32x32x16_bf16 v[32:47], v[192:195], v[96:99], 0\n"
    "s_waitcnt lgkmcnt(0)\n"
    "v_exp_f32 v85, v85\n"
    "v_exp_f32 v86, v86\n"
    "v_exp_f32 v87, v87\n"
    "v_exp_f32 v88, v88\n"
    "v_exp_f32 v89, v89\n"
    "v_exp_f32 v90, v90\n"
    "v_smfmac_f32_32x32x32_bf16 v[0:15], v[204:207], v[72:79], v250\n"
    "s_bitcmp0_b32 s63, s40\n"
    "v_add_u32 v104, 8192, v104\n"
    "v_exp_f32 v91, v91\n"
    "v_exp_f32 v92, v92\n"
    "v_exp_f32 v93, v93\n"
    "v_exp_f32 v94, v94\n"
    "v_exp_f32 v95, v95\n"
    "v_smfmac_f32_32x32x32_bf16 v[16:31], v[200:203], v[80:87], v250\n"
    "s_cbranch_scc1 SG_\n"
    "33: s_cbranch_execz 33f\n"
    "s_cmp_eq_u32 s40, 8\n"
    "s_cbranch_scc1 TL_\n"
    "s_cmp_eq_u32 s40, 4\n"
    "s_cbranch_scc1 J0_\n"
    "s_cmp_eq_u32 s40, 6\n"
    "s_cbranch_scc1 J2_\n"
    "s_cmp_eq_u32 s40, 7\n"
    "s_cbranch_scc1 J3_\n"
    "s_cmp_gt_u32 s40, 3\n"
    "s_cbranch_scc1 J1_\n"
    "s_cmp_eq_u32 s40, 2\n"
    "s_cbranch_scc1 JK_\n"
    "s_cmp_eq_u32 s29, 0\n"
    "s_cbranch_scc1 JA_\n"
    "s_waitcnt vmcnt(0)\n"
    "s_mov_b64 exec, 1\n"
    "ds_add_rtn_u32 v112, v108, v109 offset:4\n"
    "v_mov_b64 v[240:241], s[82:83]\n"
    "v_mov_b32 v113, 0\n"
    "s_waitcnt lgkmcnt(0)\n"
    "v_cmp_eq_u32 vcc, 3, v112\n"
    "s_cbranch_vccz U27\n"
    "global_store_dwordx2 v113, v[240:241], s[52:53] sc1\n"
    "U27:\n"
    "s_mov_b64 exec, -1\n"
    "s_setprio 2\n"
    "s_branch SG_\n"
    "JA_:\n"
    "s_waitcnt vmcnt(0)\n"
    "s_mov_b64 exec, 1\n"
    "ds_add_u32 v108, v109 offset:28\n"
    "s_mov_b64 exec, -1\n"
    "s_branch SG_\n"
    "JK_:\n"
    "s_mov_b32 s60, 0x100000\n"
    "W28:\n"
    "ds_read_b32 v112, v108 offset:28\n"
    "s_sub_u32 s60, s60, 1\n"
    "s_cbranch_scc1 W28x\n"
    "s_waitcnt lgkmcnt(0)\n"
    "v_readfirstlane_b32 s59, v112\n"
    "s_cmp_lt_u32 s59, 4\n"
    "s_cbranch_scc1 W28\n"
    "W28x:\n"
    "s_branch SG_\n"
    "34: s_cbranch_execz 34f\n"
    "J1_:\n"
    "s_lshl_b32 s56, s70, 5\n"
    "s_add_u32 s56, s56, s36\n"
    "s_lshl_b32 s56, s56, 7\n"
    "s_add_u32 s56, s56, 17891328\n"
    "s_add_u32 s48, s24, s56\n"
    "s_addc_u32 s49, s25, 0\n"
    "v_mov_b64 v[240:241], s[82:83]\n"
    "v_mov_b32 v113, 0\n"
    "s_waitcnt vmcnt(0)\n"
    "s_mov_b64 exec, 1\n"
    "global_store_dwordx2 v113, v[240:241], s[48:49] sc1\n"
    "s_mov_b64 exec, -1\n"
    "s_setprio 1\n"
    "s_branch SG_\n"
    "J0_:\n"
    "s_lshl_b32 s56, s36, 6\n"
    "s_add_u32 s56, s56, s70\n"
    "s_lshl_b32 s56, s56, 11\n"
    "s_add_u32 s56, s56, 6291456\n"
    "s_add_u32 s48, s24, s56\n"
    "s_addc_u32 s49, s25, 0\n"
    "s_lshl_b32 s56, s70, 7\n"
    "s_add_u32 s56, s56, 16777216\n"
    "s_add_u32 s50, s24, s56\n"
    "s_addc_u32 s51, s25, 0\n"
    "v_and_b32 v113, 31, v106\n"
    "v_lshlrev_b32 v120, 2, v113\n"
    "s_cmp_lg_u32 s29, 0\n"
    "s_cbranch_scc1 J29\n"
    "s_cmp_lg_u32 s27, 0\n"
    "s_cbranch_scc1 J30\n"
    "v_lshrrev_b32 v114, 1, v113\n"
    "v_lshlrev_b32 v114, 4, v114\n"
    "v_and_b32 v115, 1, v113\n"
    "s_lshl_b32 s56, s37, 1\n"
    "v_add3_u32 v114, v114, v115, s56\n"
    "s_lshl_b32 s56, s30, 5\n"
    "v_add_u32 v115, s56, v113\n"
    "v_cmp_gt_u32 vcc, 32, v106\n"
    "s_nop 1\n"
    "35: s_cbranch_execz 35f\n"
    "v_cndmask_b32 v114, v114, v115, vcc\n"
    "v_lshlrev_b32 v114, 7, v114\n"
    "s_add_u32 s52, s24, 17858560\n"
    "s_addc_u32 s53, s25, 0\n"
    "s_mov_b32 s60, 0x100000\n"
    "P35:\n"
    "global_load_dwordx2 v[240:241], v114, s[52:53] sc1\n"
    "s_sub_u32 s60, s60, 1\n"
    "s_cbranch_scc1 P35x\n"
    "s_waitcnt vmcnt(0)\n"
    "v_cmp_ne_u64 vcc, s[82:83], v[240:241]\n"
    "s_cbranch_vccnz P35\n"
    "P35x:\n"
    "s_mov_b64 exec, 1\n"
    "ds_add_u32 v108, v109 offset:12\n"
    "s_mov_b64 exec, -1\n"
    "s_branch J31\n"
    "J30:\n"
    "s_mov_b32 s60, 0x100000\n"
    "W36:\n"
    "ds_read_b32 v112, v108 offset:12\n"
    "s_sub_u32 s60, s60, 1\n"
    "s_cbranch_scc1 W36x\n"
    "s_waitcnt lgkmcnt(0)\n"
    "v_readfirstlane_b32 s59, v112\n"
    "s_cmp_lt_u32 s59, 1\n"
    "s_cbranch_scc1 W36\n"
    "W36x:\n"
    "s_branch J31\n"
    "J29:\n"
    "s_mov_b32 s60, 0x100000\n"
    "W37:\n"
    "ds_read_b32 v112, v108 offset:40\n"
    "s_sub_u32 s60, s60, 1\n"
    "s_cbranch_scc1 W37x\n"
    "s_waitcnt lgkmcnt(0)\n"
    "v_readfirstlane_b32 s59, v112\n"
    "s_cmp_lt_u32 s59, 4\n"
    "s_cbranch_scc1 W37\n"
    "W37x:\n"
    "J31:\n"
    "global_load_dwordx4 v[112:115], v105, s[48:49] sc1\n"
    "global_load_dwordx4 v[116:119], v105, s[48:49] offset:1024 sc1\n"
    "global_load_dword v107, v120, s[50:51] sc1\n"
    "s_lshl_b32 s56, s70, 16\n"
    "36: s_cbranch_execz 36f\n"
    "s_lshl_b32 s57, s36, 10\n"
    "s_add_u32 s56, s56, s57\n"
    "s_add_u32 s56, s56, 10485760\n"
    "s_add_u32 s50, s24, s56\n"
    "s_addc_u32 s51, s25, 0\n"
    "s_add_u32 s52, s50, 32768\n"
    "s_addc_u32 s53, s51, 0\n"
    "s_cmp_lg_u32 s29, 0\n"
    "s_cbranch_scc1 J32\n"
    "s_mul_i32 s56, s36, 34816\n"
    "s_mul_i32 s57, s28, 544\n"
    "s_add_u32 s56, s56, s57\n"
    "s_add_u32 s56, s56, 14682240\n"
    "s_add_u32 s48, s24, s56\n"
    "s_addc_u32 s49, s25, 0\n"
    "v_min_u32 v120, 528, v105\n"
    "global_load_dwordx4 v[128:131], v120, s[48:49] offset:-2176 sc1\n"
    "global_load_dwordx4 v[132:135], v120, s[48:49] offset:0 sc1\n"
    "global_load_dwordx4 v[136:139], v120, s[48:49] offset:2176 sc1\n"
    "s_add_u32 s48, s48, 6528\n"
    "s_addc_u32 s49, s49, 0\n"
    "global_load_dwordx4 v[140:143], v120, s[48:49] offset:-2176 sc1\n"
    "global_load_dwordx4 v[144:147], v120, s[48:49] offset:0 sc1\n"
    "global_load_dwordx4 v[148:151], v120, s[48:49] offset:2176 sc1\n"
    "s_add_u32 s48, s48, 6528\n"
    "s_addc_u32 s49, s49, 0\n"
    "global_load_dwordx4 v[152:155], v120, s[48:49] offset:-2176 sc1\n"
    "global_load_dwordx4 v[156:159], v120, s[48:49] offset:0 sc1\n"
    "global_load_dwordx4 v[160:163], v120, s[48:49] offset:2176 sc1\n"
    "s_add_u32 s48, s48, 6528\n"
    "s_addc_u32 s49, s49, 0\n"
    "global_load_dwordx4 v[164:167], v120, s[48:49] offset:-2176 sc1\n"
    "global_load_dwordx4 v[168:171], v120, s[48:49] offset:0 sc1\n"
    "37: s_cbranch_execz 37f\n"
    "global_load_dwordx4 v[172:175], v120, s[48:49] offset:2176 sc1\n"
    "s_add_u32 s48, s48, 6528\n"
    "s_addc_u32 s49, s49, 0\n"
    "global_load_dwordx4 v[176:179], v120, s[48:49] offset:-2176 sc1\n"
    "global_load_dwordx4 v[180:183], v120, s[48:49] offset:0 sc1\n"
    "global_load_dwordx4 v[184:187], v120, s[48:49] offset:2176 sc1\n"
    "s_add_u32 s48, s48, 6528\n"
    "s_addc_u32 s49, s49, 0\n"
    "global_load_dwordx4 v[188:191], v120, s[48:49] offset:-2176 sc1\n"
    "v_cmp_lt_u32 vcc, 16, v106\n"
    "v_subrev_u32 v121, 17, v106\n"
    "s_nop 0\n"
    "v_cndmask_b32 v121, v106, v121, vcc\n"
    "v_cndmask_b32 v122, 0, 8, vcc\n"
    "v_lshl_add_u32 v122, v121, 4, v122\n"
    "s_and_b32 s56, s28, 1\n"
    "s_lshl_b32 s56, s56, 10\n"
    "s_lshr_b32 s57, s28, 1\n"
    "s_lshl_b32 s57, s57, 9\n"
    "s_add_u32 s56, s56, s57\n"
    "s_add_u32 s56, s56, 159744\n"
    "v_add_u32 v122, s56, v122\n"
    "s_waitcnt vmcnt(0)\n"
    "v_pk_add_f32 v[128:129], v[128:129], v[132:133]\n"
    "v_pk_add_f32 v[130:131], v[130:131], v[134:135]\n"
    "v_pk_add_f32 v[136:137], v[136:137], v[140:141]\n"
    "v_pk_add_f32 v[138:139], v[138:139], v[142:143]\n"
    "v_pk_add_f32 v[144:145], v[144:145], v[148:149]\n"
    "v_pk_add_f32 v[146:147], v[146:147], v[150:151]\n"
    "v_pk_add_f32 v[152:153], v[152:153], v[156:157]\n"
    "v_pk_add_f32 v[154:155], v[154:155], v[158:159]\n"
    "34: s_cbranch_execz 34f\n"
    "v_pk_add_f32 v[160:161], v[160:161], v[164:165]\n"
    "v_pk_add_f32 v[162:163], v[162:163], v[166:167]\n"
    "v_pk_add_f32 v[168:169], v[168:169], v[172:173]\n"
    "v_pk_add_f32 v[170:171], v[170:171], v[174:175]\n"
    "v_pk_add_f32 v[176:177], v[176:177], v[180:181]\n"
    "v_pk_add_f32 v[178:179], v[178:179], v[182:183]\n"
    "v_pk_add_f32 v[184:185], v[184:185], v[188:189]\n"
    "v_pk_add_f32 v[186:187], v[186:187], v[190:191]\n"
    "v_pk_add_f32 v[128:129], v[128:129], v[136:137]\n"
    "v_pk_add_f32 v[130:131], v[130:131], v[138:139]\n"
    "v_pk_add_f32 v[144:145], v[144:145], v[152:153]\n"
    "v_pk_add_f32 v[146:147], v[146:147], v[154:155]\n"
    "v_pk_add_f32 v[160:161], v[160:161], v[168:169]\n"
    "v_pk_add_f32 v[162:163], v[162:163], v[170:171]\n"
    "v_pk_add_f32 v[176:177], v[176:177], v[184:185]\n"
    "v_pk_add_f32 v[178:179], v[178:179], v[186:187]\n"
    "v_pk_add_f32 v[128:129], v[128:129], v[144:145]\n"
    "v_pk_add_f32 v[130:131], v[130:131], v[146:147]\n"
    "v_pk_add_f32 v[160:161], v[160:161], v[176:177]\n"
    "v_pk_add_f32 v[162:163], v[162:163], v[178:179]\n"
    "v_pk_add_f32 v[128:129], v[128:129], v[160:161]\n"
    "v_pk_add_f32 v[130:131], v[130:131], v[162:163]\n"
    "v_cvt_pk_bf16_f32 v128, v128, v129\n"
    "v_cvt_pk_bf16_f32 v129, v130, v131\n"
    "s_mov_b32 s48, -1\n"
    "s_mov_b32 s49, 3\n"
    "s_mov_b64 exec, s[48:49]\n"
    "ds_write_b64 v122, v[128:129]\n"
    "s_mov_b32 s48, 0x10000\n"
    "s_mov_b32 s49, 2\n"
    "s_mov_b64 exec, s[48:49]\n"
    "ds_write_b64 v122, v[128:129] offset:64\n"
    "s_mov_b64 exec, 1\n"
    "ds_add_u32 v108, v109 offset:40\n"
    "s_mov_b64 exec, -1\n"
    "s_mov_b32 s60, 0x100000\n"
    "W38:\n"
    "ds_read_b32 v120, v108 offset:40\n"
    "s_sub_u32 s60, s60, 1\n"
    "s_cbranch_scc1 W38x\n"
    "s_waitcnt lgkmcnt(0)\n"
    "v_readfirstlane_b32 s59, v120\n"
    "s_cmp_lt_u32 s59, 4\n"
    "s_cbranch_scc1 W38\n"
    "W38x:\n"
    "35: s_cbranch_execz 35f\n"
    "J32:\n"
    "v_add_u32 v120, 159744, v105\n"
    "ds_read_b128 v[124:127], v120 offset:1024\n"
    "ds_read_b128 v[120:123], v120\n"
    "s_waitcnt vmcnt(0) lgkmcnt(0)\n"
    "v_mfma_f32_32x32x16_bf16 v[128:143], v[120:123], v[112:115], 0\n"
    "v_mfma_f32_32x32x16_bf16 v[128:143], v[124:127], v[116:119], v[128:143]\n"
    "v_sub_f32 v113, 1.0, v107\n"
    "v_mov_b32 v116, -1\n"
    "v_mov_b32 v117, -1\n"
    "v_mov_b32 v118, -1\n"
    "v_mov_b32 v119, -1\n"
    "s_nop 6\n"
    "v_add_f32 v112, 0x358637bd, v136\n"
    "v_rcp_f32 v112, v112\n"
    "s_nop 0\n"
    "v_mul_f32 v112, v112, v113\n"
    "v_pk_mul_f32 v[128:129], v[128:129], v[112:113] op_sel_hi:[1,0]\n"
    "v_pk_mul_f32 v[130:131], v[130:131], v[112:113] op_sel_hi:[1,0]\n"
    "v_pk_mul_f32 v[132:133], v[132:133], v[112:113] op_sel_hi:[1,0]\n"
    "v_pk_mul_f32 v[134:135], v[134:135], v[112:113] op_sel_hi:[1,0]\n"
    "v_cvt_pk_bf16_f32 v128, v128, v129\n"
    "v_cvt_pk_bf16_f32 v129, v130, v131\n"
    "v_cvt_pk_bf16_f32 v130, v132, v133\n"
    "v_cvt_pk_bf16_f32 v131, v134, v135\n"
    "global_store_dwordx4 v105, v[128:131], s[52:53] sc1\n"
    "global_store_dwordx4 v105, v[116:119], s[50:51] sc1\n"
    "s_cmp_eq_u32 s29, 1\n"
    "s_cbranch_scc1 J1_\n"
    "s_setprio 1\n"
    "s_branch SG_\n"
    "J2_:\n"
    "s_lshl_b32 s56, s29, 2\n"
    "v_add_u32 v113, s56, v108\n"
    "s_mov_b64 exec, 1\n"
    "ds_add_u32 v113, v109 offset:44\n"
    "s_mov_b64 exec, -1\n"
    "36: s_cbranch_execz 36f\n"
    "s_and_b32 s56, s27, 1\n"
    "s_lshl1_add_u32 s71, s36, s56\n"
    "s_lshr_b32 s58, s27, 1\n"
    "s_lshl2_add_u32 s56, s71, s58\n"
    "s_lshl_b32 s57, s56, 10\n"
    "s_add_u32 s57, s57, 17891328\n"
    "s_add_u32 s48, s24, s57\n"
    "s_addc_u32 s49, s25, 0\n"
    "s_lshl_b32 s56, s71, 16\n"
    "s_lshl_b32 s57, s58, 13\n"
    "s_add_u32 s56, s56, s57\n"
    "s_add_u32 s56, s56, 10489856\n"
    "s_add_u32 s64, s24, s56\n"
    "s_addc_u32 s65, s25, 0\n"
    "s_add_u32 s50, s64, 32768\n"
    "s_addc_u32 s51, s65, 0\n"
    "v_and_b32 v114, 7, v106\n"
    "v_lshlrev_b32 v114, 7, v114\n"
    "v_bfe_u32 v119, v106, 2, 2\n"
    "v_lshrrev_b32 v115, 5, v106\n"
    "v_lshl_or_b32 v119, v115, 2, v119\n"
    "v_mul_u32_u24 v119, 1024, v119\n"
    "v_bfe_u32 v115, v106, 4, 1\n"
    "v_lshl_add_u32 v119, v115, 6, v119\n"
    "v_and_b32 v115, 3, v106\n"
    "v_lshl_add_u32 v119, v115, 4, v119\n"
    "s_mov_b32 s60, 0x100000\n"
    "P39:\n"
    "global_load_dwordx2 v[240:241], v114, s[48:49] sc1\n"
    "s_sub_u32 s60, s60, 1\n"
    "s_cbranch_scc1 P39x\n"
    "s_waitcnt vmcnt(0)\n"
    "v_cmp_ne_u64 vcc, s[82:83], v[240:241]\n"
    "s_cbranch_vccnz P39\n"
    "P39x:\n"
    "global_load_dwordx4 v[208:211], v105, s[50:51] offset:-4096 sc1\n"
    "global_load_dwordx4 v[212:215], v105, s[50:51] offset:-3072 sc1\n"
    "37: s_cbranch_execz 37f\n"
    "global_load_dwordx4 v[216:219], v105, s[50:51] offset:-2048 sc1\n"
    "global_load_dwordx4 v[220:223], v105, s[50:51] offset:-1024 sc1\n"
    "global_load_dwordx4 v[224:227], v105, s[50:51] offset:0 sc1\n"
    "global_load_dwordx4 v[228:231], v105, s[50:51] offset:1024 sc1\n"
    "global_load_dwordx4 v[232:235], v105, s[50:51] offset:2048 sc1\n"
    "global_load_dwordx4 v[236:239], v105, s[50:51] offset:3072 sc1\n"
    "s_lshl_b32 s56, s28, 13\n"
    "s_lshl_b32 s57, s37, 7\n"
    "s_add_u32 s58, s56, s57\n"
    "s_mul_i32 s56, s29, 393216\n"
    "s_add_u32 s56, s56, s58\n"
    "s_add_u32 s48, s18, s56\n"
    "s_addc_u32 s49, s19, 0\n"
    "s_add_u32 s50, s18, 131072\n"
    "s_addc_u32 s51, s19, 0\n"
    "s_cmp_lg_u32 s29, 0\n"
    "s_cselect_b32 s50, s12, s50\n"
    "s_cselect_b32 s51, s13, s51\n"
    "s_add_u32 s50, s50, s58\n"
    "s_addc_u32 s51, s51, 0\n"
    "s_lshl_b32 s56, s29, 17\n"
    "s_add_u32 s56, s56, s58\n"
    "s_add_u32 s56, s56, 262144\n"
    "s_add_u32 s66, s12, s56\n"
    "s_addc_u32 s67, s13, 0\n"
    "s_mul_i32 s56, s28, 2304\n"
    "s_add_u32 s56, s56, 147456\n"
    "v_add_u32 v116, s56, v105\n"
    "v_lshl_add_u32 v117, v106, 2, s56\n"
    "s_lshl_b32 s56, s28, 2\n"
    "s_add_u32 s56, s56, 163712\n"
    "v_mov_b32 v118, s56\n"
    "s_lshl_b32 s56, s70, 16\n"
    "s_lshl_b32 s57, s36, 10\n"
    "s_add_u32 s56, s56, s57\n"
    "s_add_u32 s56, s56, 10485760\n"
    "s_add_u32 s68, s24, s56\n"
    "s_addc_u32 s69, s25, 0\n"
    "34: s_cbranch_execz 34f\n"
    "s_lshl_b32 s56, s29, 16\n"
    "s_lshl_b32 s57, s28, 10\n"
    "s_add_u32 s61, s56, s57\n"
    "s_add_u32 s58, s61, 0x4000\n"
    "s_mov_b32 s60, 0x100000\n"
    "W40:\n"
    "ds_read_b32 v112, v113 offset:44\n"
    "s_sub_u32 s60, s60, 1\n"
    "s_cbranch_scc1 W40x\n"
    "s_waitcnt lgkmcnt(0)\n"
    "v_readfirstlane_b32 s59, v112\n"
    "s_cmp_lt_u32 s59, 4\n"
    "s_cbranch_scc1 W40\n"
    "W40x:\n"
    "s_mov_b32 m0, s61\n"
    "s_mov_b32 s41, 4\n"
    "DL_41:\n"
    "global_load_lds_dwordx4 v119, s[48:49]\n"
    "s_add_u32 m0, m0, 0x1000\n"
    "s_add_u32 s48, s48, 32768\n"
    "s_addc_u32 s49, s49, 0\n"
    "s_sub_u32 s41, s41, 1\n"
    "s_cmp_lg_u32 s41, 0\n"
    "s_cbranch_scc1 DL_41\n"
    "s_mov_b32 m0, s58\n"
    "s_mov_b32 s41, 8\n"
    "DL_42:\n"
    "global_load_lds_dwordx4 v119, s[50:51]\n"
    "s_add_u32 m0, m0, 0x1000\n"
    "s_add_u32 s50, s50, 32768\n"
    "s_addc_u32 s51, s51, 0\n"
    "s_sub_u32 s41, s41, 1\n"
    "s_cmp_lg_u32 s41, 0\n"
    "s_cbranch_scc1 DL_42\n"
    "s_waitcnt vmcnt(12)\n"
    "s_setprio 0\n"
    "s_cmp_eq_u32 s29, 0\n"
    "s_cbranch_scc1 SG_\n"
    "s_lshl_b32 s58, s28, 10\n"
    "s_add_u32 s58, s58, 131072\n"
    "s_mov_b32 m0, s58\n"
    "s_mov_b32 s41, 4\n"
    "DL_43:\n"
    "global_load_lds_dwordx4 v119, s[66:67]\n"
    "s_add_u32 m0, m0, 0x1000\n"
    "s_add_u32 s66, s66, 32768\n"
    "s_addc_u32 s67, s67, 0\n"
    "s_sub_u32 s41, s41, 1\n"
    "s_cmp_lg_u32 s41, 0\n"
    "s_cbranch_scc1 DL_43\n"
    "35: s_cbranch_execz 35f\n"
    "s_branch SG_\n"
    "J3_:\n"
    "ds_read_b32 v249, v118\n"
    "ds_read_b128 v[240:243], v116\n"
    "ds_read_b128 v[244:247], v116 offset:1024\n"
    "ds_read_b32 v248, v117 offset:2048\n"
    "s_branch SG_\n"
    "TL_:\n"
    "v_smfmac_f32_32x32x32_bf16 v[16:31], v[204:207], v[88:95], v250\n"
    "s_cmp_eq_u32 s29, 0\n"
    "s_cbranch_scc1 J44\n"
    "v_readfirstlane_b32 s56, v249\n"
    "s_cmp_eq_u32 s56, 0\n"
    "s_cbranch_scc1 LT_\n"
    "LTX_:\n"
    "ds_write_b128 v116, v[0:3]\n"
    "ds_write_b128 v116, v[4:7] offset:1024\n"
    "ds_write_b32 v117, v8 offset:2048\n"
    "s_mov_b64 exec, 1\n"
    "ds_add_u32 v118, v109 offset:16\n"
    "s_mov_b64 exec, -1\n"
    "v_mov_b32 v96, v100\n"
    "v_mov_b32 v97, v101\n"
    "v_mov_b32 v98, v102\n"
    "v_mov_b32 v99, v103\n"
    "v_pk_add_f32 v[0:1], v[16:17], v[240:241]\n"
    "v_pk_add_f32 v[2:3], v[18:19], v[242:243]\n"
    "v_pk_add_f32 v[4:5], v[20:21], v[244:245]\n"
    "v_pk_add_f32 v[6:7], v[22:23], v[246:247]\n"
    "v_add_f32 v8, v24, v248\n"
    "s_branch TC_\n"
    "J44:\n"
    "s_nop 9\n"
    "ds_write_b128 v116, v[16:19]\n"
    "ds_write_b128 v116, v[20:23] offset:1024\n"
    "ds_write_b32 v117, v24 offset:2048\n"
    "s_mov_b64 exec, 1\n"
    "ds_add_u32 v118, v109 offset:0\n"
    "s_mov_b64 exec, -1\n"
    "36: s_cbranch_execz 36f\n"
    "s_mov_b64 exec, 1\n"
    "ds_add_u32 v108, v109 offset:52\n"
    "s_mov_b64 exec, -1\n"
    "s_add_u32 s58, s61, 0xc000\n"
    "s_mov_b32 s60, 0x100000\n"
    "W46:\n"
    "ds_read_b32 v112, v108 offset:52\n"
    "s_sub_u32 s60, s60, 1\n"
    "s_cbranch_scc1 W46x\n"
    "s_waitcnt lgkmcnt(0)\n"
    "v_readfirstlane_b32 s59, v112\n"
    "s_cmp_lt_u32 s59, 4\n"
    "s_cbranch_scc1 W46\n"
    "W46x:\n"
    "s_mov_b32 m0, s58\n"
    "s_mov_b32 s41, 4\n"
    "DL_47:\n"
    "global_load_lds_dwordx4 v119, s[66:67]\n"
    "s_add_u32 m0, m0, 0x1000\n"
    "s_add_u32 s66, s66, 32768\n"
    "s_addc_u32 s67, s67, 0\n"
    "s_sub_u32 s41, s41, 1\n"
    "s_cmp_lg_u32 s41, 0\n"
    "s_cbranch_scc1 DL_47\n"
    "s_waitcnt vmcnt(0)\n"
    "s_mov_b32 s60, 0x100000\n"
    "W48:\n"
    "ds_read_b32 v112, v118 offset:16\n"
    "s_sub_u32 s60, s60, 1\n"
    "s_cbranch_scc1 W48x\n"
    "s_waitcnt lgkmcnt(0)\n"
    "v_readfirstlane_b32 s59, v112\n"
    "s_cmp_lt_u32 s59, 1\n"
    "s_cbranch_scc1 W48\n"
    "W48x:\n"
    "ds_read_b128 v[240:243], v116\n"
    "ds_read_b128 v[244:247], v116 offset:1024\n"
    "ds_read_b32 v248, v117 offset:2048\n"
    "s_waitcnt lgkmcnt(0)\n"
    "v_pk_add_f32 v[0:1], v[0:1], v[240:241]\n"
    "v_pk_add_f32 v[2:3], v[2:3], v[242:243]\n"
    "37: s_cbranch_execz 37f\n"
    "v_pk_add_f32 v[4:5], v[4:5], v[244:245]\n"
    "v_pk_add_f32 v[6:7], v[6:7], v[246:247]\n"
    "v_add_f32 v8, v8, v248\n"
    "TC_:\n"
    "v_maximum3_f32 v16, |v0|, |v1|, |v2|\n"
    "v_maximum3_f32 v16, v16, |v3|, |v4|\n"
    "v_maximum3_f32 v16, v16, |v5|, |v6|\n"
    "s_mov_b32 s56, 0x7b800000\n"
    "v_maximum3_f32 v16, v16, |v7|, |v7|\n"
    "v_cmp_nle_f32 vcc, 0x2b800000, v8\n"
    "v_cmp_nge_f32 s[50:51], s56, v16\n"
    "s_mov_b32 s56, 0x5d800000\n"
    "s_or_b64 vcc, vcc, s[50:51]\n"
    "v_cmp_nge_f32 s[50:51], s56, v8\n"
    "v_rcp_f32 v9, v8\n"
    "s_or_b64 vcc, vcc, s[50:51]\n"
    "s_cbranch_vccnz FB_\n"
    "FBX_:\n"
    "v_mul_f32 v10, v107, v9\n"
    "v_pk_mul_f32 v[0:1], v[0:1], v[10:11] op_sel_hi:[1,0]\n"
    "v_pk_mul_f32 v[2:3], v[2:3], v[10:11] op_sel_hi:[1,0]\n"
    "v_pk_mul_f32 v[4:5], v[4:5], v[10:11] op_sel_hi:[1,0]\n"
    "v_pk_mul_f32 v[6:7], v[6:7], v[10:11] op_sel_hi:[1,0]\n"
    "v_cvt_pk_bf16_f32 v0, v0, v1\n"
    "v_cvt_pk_bf16_f32 v1, v2, v3\n"
    "v_cvt_pk_bf16_f32 v2, v4, v5\n"
    "v_cvt_pk_bf16_f32 v3, v6, v7\n"
    "v_min_u32 v0, -2, v0\n"
    "v_min_u32 v1, -2, v1\n"
    "v_min_u32 v2, -2, v2\n"
    "v_min_u32 v3, -2, v3\n"
    "34: s_cbranch_execz 34f\n"
    "global_store_dwordx4 v105, v[0:3], s[68:69] sc1\n"
    "v_bfe_u32 v120, v106, 2, 2\n"
    "v_lshlrev_b32 v120, 6, v120\n"
    "v_bfe_u32 v121, v106, 4, 1\n"
    "v_lshl_or_b32 v120, v121, 5, v120\n"
    "v_and_b32 v121, 3, v106\n"
    "v_lshl_or_b32 v120, v121, 3, v120\n"
    "v_lshrrev_b32 v121, 5, v106\n"
    "v_lshl_or_b32 v120, v121, 9, v120\n"
    "s_lshr_b32 s58, s27, 1\n"
    "s_lshl_b32 s56, s58, 14\n"
    "s_cmp_eq_u32 s58, 3\n"
    "s_cselect_b32 s62, 0x10000, s56\n"
    "v_add_u32 v121, s62, v120\n"
    "s_add_u32 s56, s56, 0x14000\n"
    "s_cmp_eq_u32 s58, 2\n"
    "s_cselect_b32 s56, 0xc000, s56\n"
    "v_add_u32 v120, s56, v120\n"
    "s_waitcnt vmcnt(1)\n"
    "s_barrier\n"
    "ds_read_b64_tr_b16 v[128:129], v121 offset:0\n"
    "ds_read_b64_tr_b16 v[130:131], v121 offset:1024\n"
    "ds_read_b64_tr_b16 v[132:133], v121 offset:256\n"
    "ds_read_b64_tr_b16 v[134:135], v121 offset:1280\n"
    "ds_read_b64_tr_b16 v[136:137], v121 offset:2048\n"
    "ds_read_b64_tr_b16 v[138:139], v121 offset:3072\n"
    "ds_read_b64_tr_b16 v[140:141], v121 offset:2304\n"
    "ds_read_b64_tr_b16 v[142:143], v121 offset:3328\n"
    "ds_read_b64_tr_b16 v[144:145], v121 offset:4096\n"
    "35: s_cbranch_execz 35f\n"
    "ds_read_b64_tr_b16 v[146:147], v121 offset:5120\n"
    "ds_read_b64_tr_b16 v[148:149], v121 offset:4352\n"
    "ds_read_b64_tr_b16 v[150:151], v121 offset:5376\n"
    "s_waitcnt lgkmcnt(8)\n"
    "v_mfma_f32_32x32x16_bf16 v[32:47], v[128:131], v[208:211], 0\n"
    "v_mfma_f32_32x32x16_bf16 v[48:63], v[132:135], v[208:211], 0\n"
    "ds_read_b64_tr_b16 v[152:153], v121 offset:6144\n"
    "ds_read_b64_tr_b16 v[154:155], v121 offset:7168\n"
    "ds_read_b64_tr_b16 v[156:157], v121 offset:6400\n"
    "ds_read_b64_tr_b16 v[158:159], v121 offset:7424\n"
    "v_and_b32 v122, 31, v106\n"
    "s_and_b32 s56, s27, 1\n"
    "s_lshl_b32 s56, s56, 13\n"
    "s_add_u32 s56, s56, s62\n"
    "s_waitcnt lgkmcnt(8)\n"
    "v_mfma_f32_32x32x16_bf16 v[32:47], v[136:139], v[212:215], v[32:47]\n"
    "v_mfma_f32_32x32x16_bf16 v[48:63], v[140:143], v[212:215], v[48:63]\n"
    "ds_read_b64_tr_b16 v[160:161], v121 offset:8192\n"
    "ds_read_b64_tr_b16 v[162:163], v121 offset:9216\n"
    "ds_read_b64_tr_b16 v[164:165], v121 offset:8448\n"
    "ds_read_b64_tr_b16 v[166:167], v121 offset:9472\n"
    "v_and_b32 v123, 15, v122\n"
    "v_lshrrev_b32 v124, 5, v106\n"
    "v_xor_b32 v123, v123, v124\n"
    "v_lshlrev_b32 v123, 4, v123\n"
    "v_lshl_or_b32 v122, v122, 8, v123\n"
    "s_waitcnt lgkmcnt(8)\n"
    "v_mfma_f32_32x32x16_bf16 v[32:47], v[144:147], v[216:219], v[32:47]\n"
    "v_mfma_f32_32x32x16_bf16 v[48:63], v[148:151], v[216:219], v[48:63]\n"
    "36: s_cbranch_execz 36f\n"
    "ds_read_b64_tr_b16 v[168:169], v121 offset:10240\n"
    "ds_read_b64_tr_b16 v[170:171], v121 offset:11264\n"
    "ds_read_b64_tr_b16 v[172:173], v121 offset:10496\n"
    "ds_read_b64_tr_b16 v[174:175], v121 offset:11520\n"
    "v_add_u32 v122, s56, v122\n"
    "s_lshl_b32 s56, s27, 3\n"
    "v_lshrrev_b32 v127, 3, v106\n"
    "v_add_u32 v127, s56, v127\n"
    "s_waitcnt lgkmcnt(8)\n"
    "v_mfma_f32_32x32x16_bf16 v[32:47], v[152:155], v[220:223], v[32:47]\n"
    "v_mfma_f32_32x32x16_bf16 v[48:63], v[156:159], v[220:223], v[48:63]\n"
    "ds_read_b64_tr_b16 v[176:177], v121 offset:12288\n"
    "ds_read_b64_tr_b16 v[178:179], v121 offset:13312\n"
    "ds_read_b64_tr_b16 v[180:181], v121 offset:12544\n"
    "ds_read_b64_tr_b16 v[182:183], v121 offset:13568\n"
    "v_and_b32 v126, 7, v106\n"
    "v_lshlrev_b32 v123, 1, v126\n"
    "v_and_b32 v124, 15, v127\n"
    "v_xor_b32 v123, v123, v124\n"
    "v_lshlrev_b32 v123, 4, v123\n"
    "s_waitcnt lgkmcnt(8)\n"
    "v_mfma_f32_32x32x16_bf16 v[32:47], v[160:163], v[224:227], v[32:47]\n"
    "v_mfma_f32_32x32x16_bf16 v[48:63], v[164:167], v[224:227], v[48:63]\n"
    "ds_read_b64_tr_b16 v[184:185], v121 offset:14336\n"
    "ds_read_b64_tr_b16 v[186:187], v121 offset:15360\n"
    "ds_read_b64_tr_b16 v[188:189], v121 offset:14592\n"
    "ds_read_b64_tr_b16 v[190:191], v121 offset:15616\n"
    "v_lshl_or_b32 v123, v127, 8, v123\n"
    "37: s_cbranch_execz 37f\n"
    "v_xor_b32 v124, 16, v123\n"
    "v_lshlrev_b32 v126, 4, v126\n"
    "v_lshl_or_b32 v127, v127, 10, v126\n"
    "v_add_u32 v125, 0x10000, v123\n"
    "s_waitcnt lgkmcnt(8)\n"
    "v_mfma_f32_32x32x16_bf16 v[32:47], v[168:171], v[228:231], v[32:47]\n"
    "v_mfma_f32_32x32x16_bf16 v[48:63], v[172:175], v[228:231], v[48:63]\n"
    "v_add_u32 v126, 0x10000, v124\n"
    "s_lshl_b32 s56, s36, 16\n"
    "s_lshl_b32 s57, s37, 7\n"
    "s_add_u32 s56, s56, s57\n"
    "s_waitcnt lgkmcnt(4)\n"
    "v_mfma_f32_32x32x16_bf16 v[32:47], v[176:179], v[232:235], v[32:47]\n"
    "v_mfma_f32_32x32x16_bf16 v[48:63], v[180:183], v[232:235], v[48:63]\n"
    "s_add_u32 s22, s22, s56\n"
    "s_addc_u32 s23, s23, 0\n"
    "v_xor_b32 v96, 0, v122\n"
    "v_xor_b32 v97, 32, v122\n"
    "v_xor_b32 v98, 64, v122\n"
    "s_waitcnt lgkmcnt(0)\n"
    "v_mfma_f32_32x32x16_bf16 v[32:47], v[184:187], v[236:239], v[32:47]\n"
    "v_mfma_f32_32x32x16_bf16 v[48:63], v[188:191], v[236:239], v[48:63]\n"
    "v_xor_b32 v99, 96, v122\n"
    "v_xor_b32 v100, 128, v122\n"
    "v_xor_b32 v101, 160, v122\n"
    "v_xor_b32 v102, 192, v122\n"
    "v_xor_b32 v103, 224, v122\n"
    "global_load_dwordx4 v[64:67], v105, s[64:65] offset:-4096 sc1\n"
    "global_load_dwordx4 v[68:71], v105, s[64:65] offset:-3072 sc1\n"
    "global_load_dwordx4 v[72:75], v105, s[64:65] offset:-2048 sc1\n"
    "34: s_cbranch_execz 34f\n"
    "global_load_dwordx4 v[76:79], v105, s[64:65] offset:-1024 sc1\n"
    "global_load_dwordx4 v[80:83], v105, s[64:65] offset:0 sc1\n"
    "global_load_dwordx4 v[84:87], v105, s[64:65] offset:1024 sc1\n"
    "global_load_dwordx4 v[88:91], v105, s[64:65] offset:2048 sc1\n"
    "global_load_dwordx4 v[92:95], v105, s[64:65] offset:3072 sc1\n"
    "s_barrier\n"
    "ds_read_b64_tr_b16 v[128:129], v120 offset:0\n"
    "ds_read_b64_tr_b16 v[130:131], v120 offset:1024\n"
    "ds_read_b64_tr_b16 v[132:133], v120 offset:256\n"
    "ds_read_b64_tr_b16 v[134:135], v120 offset:1280\n"
    "ds_read_b64_tr_b16 v[136:137], v120 offset:2048\n"
    "ds_read_b64_tr_b16 v[138:139], v120 offset:3072\n"
    "ds_read_b64_tr_b16 v[140:141], v120 offset:2304\n"
    "ds_read_b64_tr_b16 v[142:143], v120 offset:3328\n"
    "ds_read_b64_tr_b16 v[144:145], v120 offset:4096\n"
    "ds_read_b64_tr_b16 v[146:147], v120 offset:5120\n"
    "ds_read_b64_tr_b16 v[148:149], v120 offset:4352\n"
    "ds_read_b64_tr_b16 v[150:151], v120 offset:5376\n"
    "ds_read_b64_tr_b16 v[152:153], v120 offset:6144\n"
    "ds_read_b64_tr_b16 v[154:155], v120 offset:7168\n"
    "ds_read_b64_tr_b16 v[156:157], v120 offset:6400\n"
    "ds_read_b64_tr_b16 v[158:159], v120 offset:7424\n"
    "ds_read_b64_tr_b16 v[160:161], v120 offset:8192\n"
    "ds_read_b64_tr_b16 v[162:163], v120 offset:9216\n"
    "ds_read_b64_tr_b16 v[164:165], v120 offset:8448\n"
    "ds_read_b64_tr_b16 v[166:167], v120 offset:9472\n"
    "ds_read_b64_tr_b16 v[168:169], v120 offset:10240\n"
    "35: s_cbranch_execz 35f\n"
    "ds_read_b64_tr_b16 v[170:171], v120 offset:11264\n"
    "ds_read_b64_tr_b16 v[172:173], v120 offset:10496\n"
    "ds_read_b64_tr_b16 v[174:175], v120 offset:11520\n"
    "ds_read_b64_tr_b16 v[176:177], v120 offset:12288\n"
    "ds_read_b64_tr_b16 v[178:179], v120 offset:13312\n"
    "ds_read_b64_tr_b16 v[180:181], v120 offset:12544\n"
    "ds_read_b64_tr_b16 v[182:183], v120 offset:13568\n"
    "ds_read_b64_tr_b16 v[184:185], v120 offset:14336\n"
    "ds_read_b64_tr_b16 v[186:187], v120 offset:15360\n"
    "ds_read_b64_tr_b16 v[188:189], v120 offset:14592\n"
    "ds_read_b64_tr_b16 v[190:191], v120 offset:15616\n"
    "s_mov_b32 s60, 0x10000\n"
    "s_waitcnt lgkmcnt(0)\n"
    "RT_:\n"
    "v_mov_b32 v104, 0\n"
    "s_waitcnt vmcnt(7)\n"
    "v_mfma_f32_32x32x16_bf16 v[0:15], v[128:131], v[64:67], v[32:47]\n"
    "v_max3_u32 v104, v104, v64, v65\n"
    "v_mfma_f32_32x32x16_bf16 v[16:31], v[132:135], v[64:67], v[48:63]\n"
    "v_max3_u32 v104, v104, v66, v67\n"
    "s_waitcnt vmcnt(6)\n"
    "v_mfma_f32_32x32x16_bf16 v[0:15], v[136:139], v[68:71], v[0:15]\n"
    "v_max3_u32 v104, v104, v68, v69\n"
    "v_mfma_f32_32x32x16_bf16 v[16:31], v[140:143], v[68:71], v[16:31]\n"
    "v_max3_u32 v104, v104, v70, v71\n"
    "36: s_cbranch_execz 36f\n"
    "s_waitcnt vmcnt(5)\n"
    "v_mfma_f32_32x32x16_bf16 v[0:15], v[144:147], v[72:75], v[0:15]\n"
    "v_max3_u32 v104, v104, v72, v73\n"
    "v_mfma_f32_32x32x16_bf16 v[16:31], v[148:151], v[72:75], v[16:31]\n"
    "v_max3_u32 v104, v104, v74, v75\n"
    "s_waitcnt vmcnt(4)\n"
    "v_mfma_f32_32x32x16_bf16 v[0:15], v[152:155], v[76:79], v[0:15]\n"
    "v_max3_u32 v104, v104, v76, v77\n"
    "v_mfma_f32_32x32x16_bf16 v[16:31], v[156:159], v[76:79], v[16:31]\n"
    "v_max3_u32 v104, v104, v78, v79\n"
    "s_waitcnt vmcnt(3)\n"
    "v_mfma_f32_32x32x16_bf16 v[0:15], v[160:163], v[80:83], v[0:15]\n"
    "v_max3_u32 v104, v104, v80, v81\n"
    "v_mfma_f32_32x32x16_bf16 v[16:31], v[164:167], v[80:83], v[16:31]\n"
    "v_max3_u32 v104, v104, v82, v83\n"
    "s_waitcnt vmcnt(2)\n"
    "v_mfma_f32_32x32x16_bf16 v[0:15], v[168:171], v[84:87], v[0:15]\n"
    "v_max3_u32 v104, v104, v84, v85\n"
    "v_mfma_f32_32x32x16_bf16 v[16:31], v[172:175], v[84:87], v[16:31]\n"
    "v_max3_u32 v104, v104, v86, v87\n"
    "s_waitcnt vmcnt(1)\n"
    "v_mfma_f32_32x32x16_bf16 v[0:15], v[176:179], v[88:91], v[0:15]\n"
    "v_max3_u32 v104, v104, v88, v89\n"
    "v_mfma_f32_32x32x16_bf16 v[16:31], v[180:183], v[88:91], v[16:31]\n"
    "v_max3_u32 v104, v104, v90, v91\n"
    "37: s_cbranch_execz 37f\n"
    "s_waitcnt vmcnt(0)\n"
    "v_mfma_f32_32x32x16_bf16 v[0:15], v[184:187], v[92:95], v[0:15]\n"
    "v_max3_u32 v104, v104, v92, v93\n"
    "v_mfma_f32_32x32x16_bf16 v[16:31], v[188:191], v[92:95], v[16:31]\n"
    "v_max3_u32 v104, v104, v94, v95\n"
    "v_cmp_eq_u32 vcc, -1, v104\n"
    "s_cbranch_vccz J45\n"
    "s_sub_u32 s60, s60, 1\n"
    "s_cbranch_scc1 J45\n"
    "global_load_dwordx4 v[64:67], v105, s[64:65] offset:-4096 sc1\n"
    "global_load_dwordx4 v[68:71], v105, s[64:65] offset:-3072 sc1\n"
    "global_load_dwordx4 v[72:75], v105, s[64:65] offset:-2048 sc1\n"
    "global_load_dwordx4 v[76:79], v105, s[64:65] offset:-1024 sc1\n"
    "global_load_dwordx4 v[80:83], v105, s[64:65] offset:0 sc1\n"
    "global_load_dwordx4 v[84:87], v105, s[64:65] offset:1024 sc1\n"
    "global_load_dwordx4 v[88:91], v105, s[64:65] offset:2048 sc1\n"
    "global_load_dwordx4 v[92:95], v105, s[64:65] offset:3072 sc1\n"
    "s_branch RT_\n"
    "J45:\n"
    "s_nop 8\n"
    "s_or_b32 s56, s26, s27\n"
    "s_cbranch_scc1 EN_\n"
    "s_add_u32 s48, s24, 18874368\n"
    "s_addc_u32 s49, s25, 0\n"
    "v_mov_b32 v104, s82\n"
    "v_mov_b32 v112, 0\n"
    "s_mov_b64 exec, 1\n"
    "global_store_dword v112, v104, s[48:49] sc1\n"
    "s_mov_b64 exec, -1\n"
    "EN_:\n"
    "ds_write_b128 v96, v[0:3]\n"
    "ds_write_b128 v97, v[4:7]\n"
    "ds_write_b128 v98, v[8:11]\n"
    "ds_write_b128 v99, v[12:15]\n"
    "ds_write_b128 v100, v[16:19]\n"
    "34: s_cbranch_execz 34f\n"
    "ds_write_b128 v101, v[20:23]\n"
    "ds_write_b128 v102, v[24:27]\n"
    "ds_write_b128 v103, v[28:31]\n"
    "s_waitcnt lgkmcnt(0)\n"
    "s_barrier\n"
    "ds_read_b128 v[0:3], v123 offset:0\n"
    "ds_read_b128 v[4:7], v124 offset:0\n"
    "ds_read_b128 v[8:11], v123 offset:16384\n"
    "ds_read_b128 v[12:15], v124 offset:16384\n"
    "ds_read_b128 v[16:19], v123 offset:32768\n"
    "ds_read_b128 v[20:23], v124 offset:32768\n"
    "ds_read_b128 v[24:27], v125 offset:0\n"
    "ds_read_b128 v[28:31], v126 offset:0\n"
    "s_waitcnt lgkmcnt(4)\n"
    "v_pk_add_f32 v[0:1], v[0:1], v[8:9]\n"
    "v_pk_add_f32 v[2:3], v[2:3], v[10:11]\n"
    "v_pk_add_f32 v[4:5], v[4:5], v[12:13]\n"
    "v_pk_add_f32 v[6:7], v[6:7], v[14:15]\n"
    "s_waitcnt lgkmcnt(2)\n"
    "v_pk_add_f32 v[0:1], v[0:1], v[16:17]\n"
    "v_pk_add_f32 v[2:3], v[2:3], v[18:19]\n"
    "v_pk_add_f32 v[4:5], v[4:5], v[20:21]\n"
    "v_pk_add_f32 v[6:7], v[6:7], v[22:23]\n"
    "s_waitcnt lgkmcnt(0)\n"
    "v_pk_add_f32 v[0:1], v[0:1], v[24:25]\n"
    "v_pk_add_f32 v[2:3], v[2:3], v[26:27]\n"
    "v_pk_add_f32 v[4:5], v[4:5], v[28:29]\n"
    "v_pk_add_f32 v[6:7], v[6:7], v[30:31]\n"
    "v_cvt_pk_bf16_f32 v0, v0, v1\n"
    "35: s_cbranch_execz 35f\n"
    "v_cvt_pk_bf16_f32 v1, v2, v3\n"
    "v_cvt_pk_bf16_f32 v2, v4, v5\n"
    "v_cvt_pk_bf16_f32 v3, v6, v7\n"
    "global_store_dwordx4 v127, v[0:3], s[22:23] sc0 sc1\n"
    "s_endpgm\n"
    "10:\n"
    "11:\n"
    "12:\n"
    "13:\n"
    "14:\n"
    "15:\n"
    "16:\n"
    "17:\n"
    "22:\n"
    "23:\n"
    "24:\n"
    "25:\n"
    "26:\n"
    "27:\n"
    "28:\n"
    "29:\n"
    "30:\n"
    "31:\n"
    "32:\n"
    "33:\n"
    "34:\n"
    "35:\n"
    "36:\n"
    "37:\n"
    "s_setpc_b64 s[44:45]\n"
    "LT_:\n"
    "s_mov_b32 s60, 0x100000\n"
    "W49:\n"
    "ds_read_b32 v112, v118 offset:0\n"
    "s_sub_u32 s60, s60, 1\n"
    "s_cbranch_scc1 W49x\n"
    "s_waitcnt lgkmcnt(0)\n"
    "v_readfirstlane_b32 s59, v112\n"
    "s_cmp_lt_u32 s59, 1\n"
    "s_cbranch_scc1 W49\n"
    "W49x:\n"
    "ds_read_b128 v[240:243], v116\n"
    "ds_read_b128 v[244:247], v116 offset:1024\n"
    "ds_read_b32 v248, v117 offset:2048\n"
    "s_waitcnt lgkmcnt(0)\n"
    "s_branch LTX_\n"
    "FB_:\n"
    "s_lshl_b32 s56, s36, 17\n"
    "s_add_u32 s56, s56, 2097152\n"
    "s_add_u32 s50, s24, s56\n"
    "s_addc_u32 s51, s25, 0\n"
    "v_mov_b32 v0, 0\n"
    "v_mov_b32 v1, 0\n"
    "v_mov_b32 v2, 0\n"
    "v_mov_b32 v3, 0\n"
    "v_mov_b32 v4, 0\n"
    "v_mov_b32 v5, 0\n"
    "v_mov_b32 v6, 0\n"
    "v_mov_b32 v7, 0\n"
    "v_mov_b32 v8, 0\n"
    "v_mov_b32 v9, 0\n"
    "v_mov_b32 v10, 0\n"
    "v_mov_b32 v11, 0\n"
    "v_mov_b32 v12, 0\n"
    "v_mov_b32 v13, 0\n"
    "v_mov_b32 v14, 0\n"
    "v_mov_b32 v15, 0\n"
    "v_mov_b32 v16, 0xff800000\n"
    "s_mov_b32 s41, 64\n"
    "FL_:\n"
    "global_load_dwordx4 v[192:195], v105, s[50:51] sc1\n"
    "s_mov_b64 exec, s[46:47]\n"
    "global_load_dwordx4 v[196:199], v105, s[50:51] offset:1024 sc1\n"
    "global_load_dwordx4 v[204:207], v105, s[50:51] offset:1280 sc1\n"
    "s_mov_b64 exec, -1\n"
    "s_add_u32 s50, s50, 2048\n"
    "s_addc_u32 s51, s51, 0\n"
    "s_waitcnt vmcnt(0)\n"
    "v_mfma_f32_32x32x16_bf16 v[32:47], v[192:195], v[96:99], 0\n"
    "s_nop 11\n"
    "v_max3_f32 v17, v32, v33, v16\n"
    "v_max3_f32 v17, v34, v35, v17\n"
    "v_max3_f32 v17, v36, v37, v17\n"
    "v_max3_f32 v17, v38, v39, v17\n"
    "v_max3_f32 v17, v40, v41, v17\n"
    "v_max3_f32 v17, v42, v43, v17\n"
    "v_max3_f32 v17, v44, v45, v17\n"
    "v_max3_f32 v17, v46, v47, v17\n"
    "v_mov_b32 v18, v17\n"
    "s_nop 1\n"
    "v_permlane32_swap_b32 v17, v18\n"
    "v_max_f32 v17, v17, v18\n"
    "v_sub_f32 v18, v16, v17\n"
    "v_exp_f32 v18, v18\n"
    "v_mov_b32 v16, v17\n"
    "v_sub_f32 v32, v32, v17\n"
    "v_sub_f32 v33, v33, v17\n"
    "v_sub_f32 v34, v34, v17\n"
    "v_sub_f32 v35, v35, v17\n"
    "v_sub_f32 v36, v36, v17\n"
    "v_sub_f32 v37, v37, v17\n"
    "v_sub_f32 v38, v38, v17\n"
    "v_sub_f32 v39, v39, v17\n"
    "v_sub_f32 v40, v40, v17\n"
    "v_sub_f32 v41, v41, v17\n"
    "v_sub_f32 v42, v42, v17\n"
    "v_sub_f32 v43, v43, v17\n"
    "v_sub_f32 v44, v44, v17\n"
    "v_sub_f32 v45, v45, v17\n"
    "v_sub_f32 v46, v46, v17\n"
    "v_sub_f32 v47, v47, v17\n"
    "v_exp_f32 v32, v32\n"
    "v_exp_f32 v33, v33\n"
    "v_exp_f32 v34, v34\n"
    "v_exp_f32 v35, v35\n"
    "v_exp_f32 v36, v36\n"
    "v_exp_f32 v37, v37\n"
    "v_exp_f32 v38, v38\n"
    "v_exp_f32 v39, v39\n"
    "v_exp_f32 v40, v40\n"
    "v_exp_f32 v41, v41\n"
    "v_exp_f32 v42, v42\n"
    "v_exp_f32 v43, v43\n"
    "v_exp_f32 v44, v44\n"
    "v_exp_f32 v45, v45\n"
    "v_exp_f32 v46, v46\n"
    "v_exp_f32 v47, v47\n"
    "v_mul_f32 v0, v0, v18\n"
    "v_mul_f32 v1, v1, v18\n"
    "v_mul_f32 v2, v2, v18\n"
    "v_mul_f32 v3, v3, v18\n"
    "v_mul_f32 v4, v4, v18\n"
    "v_mul_f32 v5, v5, v18\n"
    "v_mul_f32 v6, v6, v18\n"
    "v_mul_f32 v7, v7, v18\n"
    "v_mul_f32 v8, v8, v18\n"
    "v_mul_f32 v9, v9, v18\n"
    "v_mul_f32 v10, v10, v18\n"
    "v_mul_f32 v11, v11, v18\n"
    "v_mul_f32 v12, v12, v18\n"
    "v_mul_f32 v13, v13, v18\n"
    "v_mul_f32 v14, v14, v18\n"
    "v_mul_f32 v15, v15, v18\n"
    "s_nop 1\n"
    "v_smfmac_f32_32x32x32_bf16 v[0:15], v[196:199], v[32:39], v250\n"
    "v_smfmac_f32_32x32x32_bf16 v[0:15], v[204:207], v[40:47], v250\n"
    "s_sub_u32 s41, s41, 1\n"
    "s_cmp_lg_u32 s41, 0\n"
    "s_nop 11\n"
    "s_cbranch_scc1 FL_\n"
    "v_rcp_f32 v9, v8\n"
    "s_nop 0\n"
    "s_branch FBX_\n"
    "PE_:\n"
    "v_mfma_f32_32x32x16_bf16 v[128:143], v[64:67], v[80:83], 0\n"
    "v_mfma_f32_32x32x16_bf16 v[144:159], v[68:71], v[84:87], 0\n"
    "v_mfma_f32_32x32x16_bf16 v[160:175], v[72:75], v[88:91], 0\n"
    "v_mfma_f32_32x32x16_bf16 v[176:191], v[76:79], v[92:95], 0\n"
    "s_nop 8\n"
    "v_max3_f32 v192, v128, v129, v130\n"
    "v_max3_f32 v196, v144, v145, v146\n"
    "v_max3_f32 v200, v160, v161, v162\n"
    "v_max3_f32 v204, v176, v177, v178\n"
    "v_max3_f32 v193, -v128, -v129, -v130\n"
    "v_max3_f32 v197, -v144, -v145, -v146\n"
    "v_max3_f32 v201, -v160, -v161, -v162\n"
    "v_max3_f32 v205, -v176, -v177, -v178\n"
    "v_max3_f32 v194, v131, v132, v133\n"
    "v_max3_f32 v198, v147, v148, v149\n"
    "v_max3_f32 v202, v163, v164, v165\n"
    "v_max3_f32 v206, v179, v180, v181\n"
    "v_max3_f32 v195, -v131, -v132, -v133\n"
    "v_max3_f32 v199, -v147, -v148, -v149\n"
    "v_max3_f32 v203, -v163, -v164, -v165\n"
    "v_max3_f32 v207, -v179, -v180, -v181\n"
    "v_max3_f32 v192, v192, v134, v135\n"
    "v_max3_f32 v196, v196, v150, v151\n"
    "v_max3_f32 v200, v200, v166, v167\n"
    "v_max3_f32 v204, v204, v182, v183\n"
    "v_max3_f32 v193, v193, -v134, -v135\n"
    "v_max3_f32 v197, v197, -v150, -v151\n"
    "v_max3_f32 v201, v201, -v166, -v167\n"
    "v_max3_f32 v205, v205, -v182, -v183\n"
    "v_max_f32 v192, v192, v194\n"
    "v_max_f32 v196, v196, v198\n"
    "v_max_f32 v200, v200, v202\n"
    "v_max_f32 v204, v204, v206\n"
    "v_max_f32 v193, v193, v195\n"
    "v_max_f32 v197, v197, v199\n"
    "v_max_f32 v201, v201, v203\n"
    "v_max_f32 v205, v205, v207\n"
    "v_permlane32_swap_b32 v192, v193\n"
    "v_permlane32_swap_b32 v196, v197\n"
    "v_permlane32_swap_b32 v200, v201\n"
    "v_permlane32_swap_b32 v204, v205\n"
    "v_max_f32 v192, v192, v193\n"
    "v_max_f32 v196, v196, v197\n"
    "v_max_f32 v200, v200, v201\n"
    "v_max_f32 v204, v204, v205\n"
    "v_mov_b32 v193, v192\n"
    "v_mov_b32 v197, v196\n"
    "v_mov_b32 v201, v200\n"
    "v_mov_b32 v205, v204\n"
    "v_permlane32_swap_b32 v192, v193\n"
    "v_permlane32_swap_b32 v196, v197\n"
    "v_permlane32_swap_b32 v200, v201\n"
    "v_permlane32_swap_b32 v204, v205\n"
    "v_pk_add_f32 v[136:137], v[128:129], v[192:193] op_sel:[0,1] op_sel_hi:[1,1] neg_lo:[1,1] neg_hi:[1,1]\n"
    "v_pk_add_f32 v[152:153], v[144:145], v[196:197] op_sel:[0,1] op_sel_hi:[1,1] neg_lo:[1,1] neg_hi:[1,1]\n"
    "v_pk_add_f32 v[168:169], v[160:161], v[200:201] op_sel:[0,1] op_sel_hi:[1,1] neg_lo:[1,1] neg_hi:[1,1]\n"
    "v_pk_add_f32 v[184:185], v[176:177], v[204:205] op_sel:[0,1] op_sel_hi:[1,1] neg_lo:[1,1] neg_hi:[1,1]\n"
    "v_pk_add_f32 v[138:139], v[130:131], v[192:193] op_sel:[0,1] op_sel_hi:[1,1] neg_lo:[1,1] neg_hi:[1,1]\n"
    "v_pk_add_f32 v[154:155], v[146:147], v[196:197] op_sel:[0,1] op_sel_hi:[1,1] neg_lo:[1,1] neg_hi:[1,1]\n"
    "v_pk_add_f32 v[170:171], v[162:163], v[200:201] op_sel:[0,1] op_sel_hi:[1,1] neg_lo:[1,1] neg_hi:[1,1]\n"
    "v_pk_add_f32 v[186:187], v[178:179], v[204:205] op_sel:[0,1] op_sel_hi:[1,1] neg_lo:[1,1] neg_hi:[1,1]\n"
    "v_pk_add_f32 v[140:141], v[132:133], v[192:193] op_sel:[0,1] op_sel_hi:[1,1] neg_lo:[1,1] neg_hi:[1,1]\n"
    "v_pk_add_f32 v[156:157], v[148:149], v[196:197] op_sel:[0,1] op_sel_hi:[1,1] neg_lo:[1,1] neg_hi:[1,1]\n"
    "v_pk_add_f32 v[172:173], v[164:165], v[200:201] op_sel:[0,1] op_sel_hi:[1,1] neg_lo:[1,1] neg_hi:[1,1]\n"
    "v_pk_add_f32 v[188:189], v[180:181], v[204:205] op_sel:[0,1] op_sel_hi:[1,1] neg_lo:[1,1] neg_hi:[1,1]\n"
    "v_pk_add_f32 v[142:143], v[134:135], v[192:193] op_sel:[0,1] op_sel_hi:[1,1] neg_lo:[1,1] neg_hi:[1,1]\n"
    "v_pk_add_f32 v[158:159], v[150:151], v[196:197] op_sel:[0,1] op_sel_hi:[1,1] neg_lo:[1,1] neg_hi:[1,1]\n"
    "v_pk_add_f32 v[174:175], v[166:167], v[200:201] op_sel:[0,1] op_sel_hi:[1,1] neg_lo:[1,1] neg_hi:[1,1]\n"
    "v_pk_add_f32 v[190:191], v[182:183], v[204:205] op_sel:[0,1] op_sel_hi:[1,1] neg_lo:[1,1] neg_hi:[1,1]\n"
    "v_pk_add_f32 v[128:129], v[128:129], v[192:193] op_sel_hi:[1,0] neg_lo:[0,1] neg_hi:[0,1]\n"
    "v_pk_add_f32 v[144:145], v[144:145], v[196:197] op_sel_hi:[1,0] neg_lo:[0,1] neg_hi:[0,1]\n"
    "v_pk_add_f32 v[160:161], v[160:161], v[200:201] op_sel_hi:[1,0] neg_lo:[0,1] neg_hi:[0,1]\n"
    "v_pk_add_f32 v[176:177], v[176:177], v[204:205] op_sel_hi:[1,0] neg_lo:[0,1] neg_hi:[0,1]\n"
    "v_pk_add_f32 v[130:131], v[130:131], v[192:193] op_sel_hi:[1,0] neg_lo:[0,1] neg_hi:[0,1]\n"
    "v_pk_add_f32 v[146:147], v[146:147], v[196:197] op_sel_hi:[1,0] neg_lo:[0,1] neg_hi:[0,1]\n"
    "v_pk_add_f32 v[162:163], v[162:163], v[200:201] op_sel_hi:[1,0] neg_lo:[0,1] neg_hi:[0,1]\n"
    "v_pk_add_f32 v[178:179], v[178:179], v[204:205] op_sel_hi:[1,0] neg_lo:[0,1] neg_hi:[0,1]\n"
    "v_pk_add_f32 v[132:133], v[132:133], v[192:193] op_sel_hi:[1,0] neg_lo:[0,1] neg_hi:[0,1]\n"
    "v_pk_add_f32 v[148:149], v[148:149], v[196:197] op_sel_hi:[1,0] neg_lo:[0,1] neg_hi:[0,1]\n"
    "v_pk_add_f32 v[164:165], v[164:165], v[200:201] op_sel_hi:[1,0] neg_lo:[0,1] neg_hi:[0,1]\n"
    "v_pk_add_f32 v[180:181], v[180:181], v[204:205] op_sel_hi:[1,0] neg_lo:[0,1] neg_hi:[0,1]\n"
    "v_pk_add_f32 v[134:135], v[134:135], v[192:193] op_sel_hi:[1,0] neg_lo:[0,1] neg_hi:[0,1]\n"
    "v_pk_add_f32 v[150:151], v[150:151], v[196:197] op_sel_hi:[1,0] neg_lo:[0,1] neg_hi:[0,1]\n"
    "v_pk_add_f32 v[166:167], v[166:167], v[200:201] op_sel_hi:[1,0] neg_lo:[0,1] neg_hi:[0,1]\n"
    "v_pk_add_f32 v[182:183], v[182:183], v[204:205] op_sel_hi:[1,0] neg_lo:[0,1] neg_hi:[0,1]\n"
    "v_exp_f32 v128, v128\n"
    "v_exp_f32 v144, v144\n"
    "v_exp_f32 v160, v160\n"
    "v_exp_f32 v176, v176\n"
    "v_exp_f32 v129, v129\n"
    "v_exp_f32 v145, v145\n"
    "v_exp_f32 v161, v161\n"
    "v_exp_f32 v177, v177\n"
    "v_exp_f32 v130, v130\n"
    "v_exp_f32 v146, v146\n"
    "v_exp_f32 v162, v162\n"
    "v_exp_f32 v178, v178\n"
    "v_exp_f32 v131, v131\n"
    "v_exp_f32 v147, v147\n"
    "v_exp_f32 v163, v163\n"
    "v_exp_f32 v179, v179\n"
    "v_exp_f32 v132, v132\n"
    "v_exp_f32 v148, v148\n"
    "v_exp_f32 v164, v164\n"
    "v_exp_f32 v180, v180\n"
    "v_exp_f32 v133, v133\n"
    "v_exp_f32 v149, v149\n"
    "v_exp_f32 v165, v165\n"
    "v_exp_f32 v181, v181\n"
    "v_exp_f32 v134, v134\n"
    "v_exp_f32 v150, v150\n"
    "v_exp_f32 v166, v166\n"
    "v_exp_f32 v182, v182\n"
    "v_exp_f32 v135, v135\n"
    "v_exp_f32 v151, v151\n"
    "v_exp_f32 v167, v167\n"
    "v_exp_f32 v183, v183\n"
    "v_exp_f32 v136, v136\n"
    "v_exp_f32 v152, v152\n"
    "v_exp_f32 v168, v168\n"
    "v_exp_f32 v184, v184\n"
    "v_exp_f32 v137, v137\n"
    "v_exp_f32 v153, v153\n"
    "v_exp_f32 v169, v169\n"
    "v_exp_f32 v185, v185\n"
    "v_exp_f32 v138, v138\n"
    "v_exp_f32 v154, v154\n"
    "v_exp_f32 v170, v170\n"
    "v_exp_f32 v186, v186\n"
    "v_exp_f32 v139, v139\n"
    "v_exp_f32 v155, v155\n"
    "v_exp_f32 v171, v171\n"
    "v_exp_f32 v187, v187\n"
    "v_exp_f32 v140, v140\n"
    "v_exp_f32 v156, v156\n"
    "v_exp_f32 v172, v172\n"
    "v_exp_f32 v188, v188\n"
    "v_exp_f32 v141, v141\n"
    "v_exp_f32 v157, v157\n"
    "v_exp_f32 v173, v173\n"
    "v_exp_f32 v189, v189\n"
    "v_exp_f32 v142, v142\n"
    "v_exp_f32 v158, v158\n"
    "v_exp_f32 v174, v174\n"
    "v_exp_f32 v190, v190\n"
    "v_exp_f32 v143, v143\n"
    "v_exp_f32 v159, v159\n"
    "v_exp_f32 v175, v175\n"
    "v_exp_f32 v191, v191\n"
    "v_pk_add_f32 v[194:195], v[136:137], v[138:139]\n"
    "v_pk_add_f32 v[198:199], v[152:153], v[154:155]\n"
    "v_pk_add_f32 v[202:203], v[168:169], v[170:171]\n"
    "v_pk_add_f32 v[206:207], v[184:185], v[186:187]\n"
    "v_pk_add_f32 v[192:193], v[128:129], v[130:131]\n"
    "v_pk_add_f32 v[196:197], v[144:145], v[146:147]\n"
    "v_pk_add_f32 v[200:201], v[160:161], v[162:163]\n"
    "v_pk_add_f32 v[204:205], v[176:177], v[178:179]\n"
    "v_pk_add_f32 v[194:195], v[194:195], v[140:141]\n"
    "v_pk_add_f32 v[198:199], v[198:199], v[156:157]\n"
    "v_pk_add_f32 v[202:203], v[202:203], v[172:173]\n"
    "v_pk_add_f32 v[206:207], v[206:207], v[188:189]\n"
    "v_pk_add_f32 v[192:193], v[192:193], v[132:133]\n"
    "v_pk_add_f32 v[196:197], v[196:197], v[148:149]\n"
    "v_pk_add_f32 v[200:201], v[200:201], v[164:165]\n"
    "v_pk_add_f32 v[204:205], v[204:205], v[180:181]\n"
    "v_pk_add_f32 v[194:195], v[194:195], v[142:143]\n"
    "v_pk_add_f32 v[198:199], v[198:199], v[158:159]\n"
    "v_pk_add_f32 v[202:203], v[202:203], v[174:175]\n"
    "v_pk_add_f32 v[206:207], v[206:207], v[190:191]\n"
    "v_pk_add_f32 v[192:193], v[192:193], v[134:135]\n"
    "v_pk_add_f32 v[196:197], v[196:197], v[150:151]\n"
    "v_pk_add_f32 v[200:201], v[200:201], v[166:167]\n"
    "v_pk_add_f32 v[204:205], v[204:205], v[182:183]\n"
    "v_add_f32 v192, v192, v193\n"
    "v_add_f32 v196, v196, v197\n"
    "v_add_f32 v200, v200, v201\n"
    "v_add_f32 v204, v204, v205\n"
    "v_add_f32 v193, v194, v195\n"
    "v_add_f32 v197, v198, v199\n"
    "v_add_f32 v201, v202, v203\n"
    "v_add_f32 v205, v206, v207\n"
    "v_permlane32_swap_b32 v192, v193\n"
    "v_permlane32_swap_b32 v196, v197\n"
    "v_permlane32_swap_b32 v200, v201\n"
    "v_permlane32_swap_b32 v204, v205\n"
    "v_add_f32 v192, v192, v193\n"
    "v_add_f32 v196, v196, v197\n"
    "v_add_f32 v200, v200, v201\n"
    "v_add_f32 v204, v204, v205\n"
    "v_rcp_f32 v192, v192\n"
    "v_rcp_f32 v196, v196\n"
    "v_rcp_f32 v200, v200\n"
    "v_rcp_f32 v204, v204\n"
    "v_mov_b32 v193, v192\n"
    "v_mov_b32 v197, v196\n"
    "v_mov_b32 v201, v200\n"
    "v_mov_b32 v205, v204\n"
    "v_permlane32_swap_b32 v192, v193\n"
    "v_permlane32_swap_b32 v196, v197\n"
    "v_permlane32_swap_b32 v200, v201\n"
    "v_permlane32_swap_b32 v204, v205\n"
    "v_pk_mul_f32 v[128:129], v[128:129], v[192:193] op_sel_hi:[1,0]\n"
    "v_pk_mul_f32 v[144:145], v[144:145], v[196:197] op_sel_hi:[1,0]\n"
    "v_pk_mul_f32 v[160:161], v[160:161], v[200:201] op_sel_hi:[1,0]\n"
    "v_pk_mul_f32 v[176:177], v[176:177], v[204:205] op_sel_hi:[1,0]\n"
    "v_pk_mul_f32 v[130:131], v[130:131], v[192:193] op_sel_hi:[1,0]\n"
    "v_pk_mul_f32 v[146:147], v[146:147], v[196:197] op_sel_hi:[1,0]\n"
    "v_pk_mul_f32 v[162:163], v[162:163], v[200:201] op_sel_hi:[1,0]\n"
    "v_pk_mul_f32 v[178:179], v[178:179], v[204:205] op_sel_hi:[1,0]\n"
    "v_pk_mul_f32 v[132:133], v[132:133], v[192:193] op_sel_hi:[1,0]\n"
    "v_pk_mul_f32 v[148:149], v[148:149], v[196:197] op_sel_hi:[1,0]\n"
    "v_pk_mul_f32 v[164:165], v[164:165], v[200:201] op_sel_hi:[1,0]\n"
    "v_pk_mul_f32 v[180:181], v[180:181], v[204:205] op_sel_hi:[1,0]\n"
    "v_pk_mul_f32 v[134:135], v[134:135], v[192:193] op_sel_hi:[1,0]\n"
    "v_pk_mul_f32 v[150:151], v[150:151], v[196:197] op_sel_hi:[1,0]\n"
    "v_pk_mul_f32 v[166:167], v[166:167], v[200:201] op_sel_hi:[1,0]\n"
    "v_pk_mul_f32 v[182:183], v[182:183], v[204:205] op_sel_hi:[1,0]\n"
    "v_pk_mul_f32 v[136:137], v[136:137], v[192:193] op_sel:[0,1] op_sel_hi:[1,1]\n"
    "v_pk_mul_f32 v[152:153], v[152:153], v[196:197] op_sel:[0,1] op_sel_hi:[1,1]\n"
    "v_pk_mul_f32 v[168:169], v[168:169], v[200:201] op_sel:[0,1] op_sel_hi:[1,1]\n"
    "v_pk_mul_f32 v[184:185], v[184:185], v[204:205] op_sel:[0,1] op_sel_hi:[1,1]\n"
    "v_pk_mul_f32 v[138:139], v[138:139], v[192:193] op_sel:[0,1] op_sel_hi:[1,1]\n"
    "v_pk_mul_f32 v[154:155], v[154:155], v[196:197] op_sel:[0,1] op_sel_hi:[1,1]\n"
    "v_pk_mul_f32 v[170:171], v[170:171], v[200:201] op_sel:[0,1] op_sel_hi:[1,1]\n"
    "v_pk_mul_f32 v[186:187], v[186:187], v[204:205] op_sel:[0,1] op_sel_hi:[1,1]\n"
    "v_pk_mul_f32 v[140:141], v[140:141], v[192:193] op_sel:[0,1] op_sel_hi:[1,1]\n"
    "v_pk_mul_f32 v[156:157], v[156:157], v[196:197] op_sel:[0,1] op_sel_hi:[1,1]\n"
    "v_pk_mul_f32 v[172:173], v[172:173], v[200:201] op_sel:[0,1] op_sel_hi:[1,1]\n"
    "v_pk_mul_f32 v[188:189], v[188:189], v[204:205] op_sel:[0,1] op_sel_hi:[1,1]\n"
    "v_pk_mul_f32 v[142:143], v[142:143], v[192:193] op_sel:[0,1] op_sel_hi:[1,1]\n"
    "v_pk_mul_f32 v[158:159], v[158:159], v[196:197] op_sel:[0,1] op_sel_hi:[1,1]\n"
    "v_pk_mul_f32 v[174:175], v[174:175], v[200:201] op_sel:[0,1] op_sel_hi:[1,1]\n"
    "v_pk_mul_f32 v[190:191], v[190:191], v[204:205] op_sel:[0,1] op_sel_hi:[1,1]\n"
    "v_cvt_pk_bf16_f32 v128, v128, v129\n"
    "v_cvt_pk_bf16_f32 v144, v144, v145\n"
    "v_cvt_pk_bf16_f32 v160, v160, v161\n"
    "v_cvt_pk_bf16_f32 v176, v176, v177\n"
    "v_cvt_pk_bf16_f32 v129, v130, v131\n"
    "v_cvt_pk_bf16_f32 v145, v146, v147\n"
    "v_cvt_pk_bf16_f32 v161, v162, v163\n"
    "v_cvt_pk_bf16_f32 v177, v178, v179\n"
    "v_cvt_pk_bf16_f32 v130, v132, v133\n"
    "v_cvt_pk_bf16_f32 v146, v148, v149\n"
    "v_cvt_pk_bf16_f32 v162, v164, v165\n"
    "v_cvt_pk_bf16_f32 v178, v180, v181\n"
    "v_cvt_pk_bf16_f32 v131, v134, v135\n"
    "v_cvt_pk_bf16_f32 v147, v150, v151\n"
    "v_cvt_pk_bf16_f32 v163, v166, v167\n"
    "v_cvt_pk_bf16_f32 v179, v182, v183\n"
    "v_cvt_pk_bf16_f32 v132, v136, v137\n"
    "v_cvt_pk_bf16_f32 v148, v152, v153\n"
    "v_cvt_pk_bf16_f32 v164, v168, v169\n"
    "v_cvt_pk_bf16_f32 v180, v184, v185\n"
    "v_cvt_pk_bf16_f32 v133, v138, v139\n"
    "v_cvt_pk_bf16_f32 v149, v154, v155\n"
    "v_cvt_pk_bf16_f32 v165, v170, v171\n"
    "v_cvt_pk_bf16_f32 v181, v186, v187\n"
    "v_cvt_pk_bf16_f32 v134, v140, v141\n"
    "v_cvt_pk_bf16_f32 v150, v156, v157\n"
    "v_cvt_pk_bf16_f32 v166, v172, v173\n"
    "v_cvt_pk_bf16_f32 v182, v188, v189\n"
    "v_cvt_pk_bf16_f32 v135, v142, v143\n"
    "v_cvt_pk_bf16_f32 v151, v158, v159\n"
    "v_cvt_pk_bf16_f32 v167, v174, v175\n"
    "v_cvt_pk_bf16_f32 v183, v190, v191\n"
    "s_nop 1\n"
    "s_setpc_b64 s[52:53]\n"
    : [tid] "+{v0}"(tid)
    : [ka] "s"(__builtin_amdgcn_kernarg_segment_ptr()), [bid] "s"(blockIdx.x), "s"((__attribute__((address_space(3))) char*)smem)
    : "v1", "v2", "v3", "v4", "v5", "v6", "v7", "v8", "v9", "v10", "v11", "v12", "v13", "v14", "v15", "v16", "v17", "v18", "v19", "v20", "v21", "v22", "v23", "v24", "v25", "v26", "v27", "v28", "v29", "v30", "v31", "v32", "v33", "v34", "v35", "v36", "v37", "v38", "v39", "v40", "v41", "v42", "v43", "v44", "v45", "v46", "v47", "v48", "v49", "v50", "v51", "v52", "v53", "v54", "v55", "v56", "v57", "v58", "v59", "v60", "v61", "v62", "v63", "v64", "v65", "v66", "v67", "v68", "v69", "v70", "v71", "v72", "v73", "v74", "v75", "v76", "v77", "v78", "v79", "v80", "v81", "v82", "v83", "v84", "v85", "v86", "v87", "v88", "v89", "v90", "v91", "v92", "v93", "v94", "v95", "v96", "v97", "v98", "v99", "v100", "v101", "v102", "v103", "v104", "v105", "v106", "v107", "v108", "v109", "v110", "v111", "v112", "v113", "v114", "v115", "v116", "v117", "v118", "v119", "v120", "v121", "v122", "v123", "v124", "v125", "v126", "v127", "v128", "v129", "v130", "v131", "v132", "v133", "v134", "v135", "v136", "v137", "v138", "v139", "v140", "v141", "v142", "v143", "v144", "v145", "v146", "v147", "v148", "v149", "v150", "v151", "v152", "v153", "v154", "v155", "v156", "v157", "v158", "v159", "v160", "v161", "v162", "v163", "v164", "v165", "v166", "v167", "v168", "v169", "v170", "v171", "v172", "v173", "v174", "v175", "v176", "v177", "v178", "v179", "v180", "v181", "v182", "v183", "v184", "v185", "v186", "v187", "v188", "v189", "v190", "v191", "v192", "v193", "v194", "v195", "v196", "v197", "v198", "v199", "v200", "v201", "v202", "v203", "v204", "v205", "v206", "v207", "v208", "v209", "v210", "v211", "v212", "v213", "v214", "v215", "v216", "v217", "v218", "v219", "v220", "v221", "v222", "v223", "v224", "v225", "v226", "v227", "v228", "v229", "v230", "v231", "v232", "v233", "v234", "v235", "v236", "v237", "v238", "v239", "v240", "v241", "v242", "v243", "v244", "v245", "v246", "v247", "v248", "v249", "v250", "v251", "v252", "v253", "v254", "v255", "s8", "s9", "s10", "s11", "s12", "s13", "s14", "s15", "s16", "s17", "s18", "s19", "s20", "s21", "s22", "s23", "s24", "s25", "s26", "s27", "s28", "s29", "s30", "s31", "s33", "s34", "s35", "s36", "s37", "s38", "s39", "s40", "s41", "s42", "s43", "s44", "s45", "s46", "s47", "s48", "s49", "s50", "s51", "s52", "s53", "s54", "s55", "s56", "s57", "s58", "s59", "s60", "s61", "s62", "s63", "s64", "s65", "s66", "s67", "s68", "s69", "s70", "s71", "s72", "s73", "s74", "s75", "s76", "s77", "s78", "s79", "s80", "s81", "s82", "s83", "s84", "s85", "s86", "s87", "vcc", "scc", "memory");
}

extern "C" void kernel_launch(void* const* d_in, const int* in_sizes, int n_in, void* d_out, int out_size, void* d_ws,
                              size_t ws_size, hipStream_t stream) {
  fused<<<256, 512, 0, stream>>>(d_in[0], d_in[1], d_in[2], d_in[4], d_in[5], d_in[6], d_in[7], d_out, d_ws);
}
```
